```python
import torch

def _softmax(x, axis=-1):
    return torch.softmax(x, dim=axis)
def _gelu(x):
    return torch.nn.functional.gelu(x, approximate="tanh")
def _silu(x):
    return torch.nn.functional.silu(x)
def _sigmoid(x):
    return torch.sigmoid(x)
def _layernorm(x, axis=-1, eps=1e-5):
    m = x.mean(dim=axis, keepdim=True)
    v = x.var(dim=axis, keepdim=True, unbiased=False)
    return (x - m) / torch.sqrt(v + eps)
def _scatter_add(tgt, idx, src, axis=0):
    return tgt.clone().index_add_(axis, idx, src)
def _segment_sum(src, seg, n):
    out = torch.zeros((n,) + src.shape[1:], dtype=src.dtype)
    return out.index_add_(0, seg, src)
def _pad(x, pw):
    flat = tuple(v for pair in reversed(pw) for v in pair)
    return torch.nn.functional.pad(x, flat)

kernel_name = 'recurrent_scan__ssm_conv_h3__mixer_s_mlp__prompt'

def setup_inputs():

    _w = (lambda a: (lambda t: t.to(torch.bfloat16) if t.is_floating_point() else t)(torch.as_tensor(a)))
    def _A_setup():
        B, S, D = 1, 2048, 1024
        ks = 4
        return {
            "q": _w((lambda *s: torch.randn(*s, dtype=torch.bfloat16) * 0.05)(B, S, D)),
            "k": _w((lambda *s: torch.randn(*s, dtype=torch.bfloat16) * 0.2)(B, S, D)),
            "v": _w((lambda *s: torch.randn(*s, dtype=torch.bfloat16) * 0.2)(B, S, D)),
            "shift_k": _w((lambda *s: torch.randn(*s, dtype=torch.bfloat16) * 0.2)(D, ks) * 0.5),
            "ssm_k": _w((lambda *s: torch.randn(*s, dtype=torch.bfloat16) * 0.2)(D, S) * (1.0 / S ** 0.5)),
            "ssm_D": _w((lambda *s: torch.randn(*s, dtype=torch.bfloat16) * 0.2)(D)),
        }
    _A_in = _A_setup()

    def _B_setup():
        B, S, D, F = 1, 2048, 1024, 3584
        return {
            "x":  _w((lambda *s: torch.randn(*s, dtype=torch.bfloat16) * 0.05)(B, S, D)),
            "w1": _w((lambda *s: torch.randn(*s, dtype=torch.bfloat16) * 1.6)(D, D) * (D ** -0.5)),
            "w2": _w((lambda *s: torch.randn(*s, dtype=torch.bfloat16) * 1.6)(D, D) * (D ** -0.5)),
            "w3": _w((lambda *s: torch.randn(*s, dtype=torch.bfloat16) * 1.6)(D, F) * (D ** -0.5)),
            "w4": _w((lambda *s: torch.randn(*s, dtype=torch.bfloat16) * 1.6)(F, D) * (F ** -0.5)),
        }
    _B_in = _B_setup()

    return {
        "A_q": _A_in["q"],
        "A_k": _A_in["k"],
        "A_v": _A_in["v"],
        "A_shift_k": _A_in["shift_k"],
        "A_ssm_k": _A_in["ssm_k"],
        "A_ssm_D": _A_in["ssm_D"],
        "B_x": _B_in["x"],
        "B_w1": _B_in["w1"],
        "B_w2": _B_in["w2"],
        "B_w3": _B_in["w3"],
        "B_w4": _B_in["w4"],
    }

def reference(A_q, A_k, A_v, A_shift_k, A_ssm_k, A_ssm_D, B_x, B_w1, B_w2, B_w3, B_w4):
    def _A_ref(q, k, v, shift_k, ssm_k, ssm_D):
        B, S, D = q.shape
        ks = shift_k.shape[-1]
        k_pad = _pad(k, ((0, 0), (ks - 1, 0), (0, 0)))
        k_shift = torch.zeros_like(k)
        for t in range(ks):
            k_shift = k_shift + k_pad[:, t:t + S, :] * shift_k[:, ks - 1 - t][None, None, :]
        kv = k_shift * v
        L = 2 * S
        kv_pad = _pad(kv, ((0, 0), (0, S), (0, 0)))
        kf_pad = _pad(ssm_k, ((0, 0), (0, S)))
        kv_f = (lambda x, axes=(-2,-1): torch.fft.rfft2(x.float(), dim=axes))(kv_pad[:, None, :, :], axes=(1, 2))
        kf_f = (lambda x, axes=(-2,-1): torch.fft.rfft2(x.float(), dim=axes))(kf_pad[:, None, :], axes=(1, 2))
        kf_f = (lambda x, *d: x.permute(*d))(kf_f, 1, 2, 0)
        y_f = kv_f * kf_f[None, :, :, :]
        y = (lambda x, s, axes=(-2,-1): torch.fft.irfft2(x, s=s, dim=axes).to(torch.bfloat16))(y_f, (1, L), axes=(1, 2))[:, 0, :S, :]
        y = y + kv * ssm_D[None, None, :]
        return q * y

    def _B_ref(x, w1, w2, w3, w4):
        B, S, D = x.shape
        h = _gelu(_layernorm(x, axis=-1) @ w1)
        c = D // 4
        g0 = _pad(h[:, :, 0*c:1*c], ((0, 0), (1, 0), (0, 0)))[:, :S, :]
        g1 = _pad(h[:, :, 1*c:2*c], ((0, 0), (0, 1), (0, 0)))[:, 1:, :]
        g2 = _pad(h[:, :, 2*c:3*c], ((0, 0), (2, 0), (0, 0)))[:, :S, :]
        g3 = _pad(h[:, :, 3*c:   ], ((0, 0), (0, 2), (0, 0)))[:, 2:, :]
        hs = torch.concatenate([g0, g1, g2, g3], axis=-1) @ w2
        x1 = x + hs
        h2 = _gelu(_layernorm(x1, axis=-1) @ w3) @ w4
        return x1 + h2

    x = A_q
    xn = _layernorm(x, axis=-1)
    s = _A_ref(xn, A_k, A_v, A_shift_k, A_ssm_k, A_ssm_D)
    g = _silu(_B_ref(xn, B_w1, B_w2, B_w3, B_w4))
    return x + s * g
```

```cpp
#include <hip/hip_runtime.h>
#include <stdint.h>

#define LAS __attribute__((address_space(3)))
#define DI __device__ __forceinline__
typedef unsigned short bf16_t;
typedef __bf16 bf16x8 __attribute__((ext_vector_type(8)));
typedef __bf16 bf16x2 __attribute__((ext_vector_type(2)));
typedef short s16x4 __attribute__((ext_vector_type(4)));
typedef short s16x8 __attribute__((ext_vector_type(8)));
typedef float f32x2 __attribute__((ext_vector_type(2)));
typedef float f32x4 __attribute__((ext_vector_type(4)));
typedef unsigned u32x2 __attribute__((ext_vector_type(2)));
typedef unsigned u32x4 __attribute__((ext_vector_type(4)));
typedef unsigned u32x4u __attribute__((ext_vector_type(4), aligned(2)));

constexpr int S = 2048, D = 1024, F = 3584;
constexpr float EPS = 1e-5f;

DI float bf_lo(unsigned w) { return __uint_as_float(w << 16); }
DI float bf_hi(unsigned w) { return __uint_as_float(w & 0xffff0000u); }
DI unsigned pack_bf16(float lo, float hi) {
  f32x2 v = {lo, hi};
  return __builtin_bit_cast(unsigned, __builtin_convertvector(v, bf16x2));
}
#define BAR() __builtin_amdgcn_s_barrier()
#define WAIT_LGKM0() asm volatile("s_waitcnt lgkmcnt(0)" ::: "memory")
template <int N> DI void wait_vm() { asm volatile("s_waitcnt vmcnt(%0)" ::"n"(N) : "memory"); }
DI void glds16(const void* src, LAS char* dst) {
  __builtin_amdgcn_global_load_lds((const unsigned*)src, (LAS unsigned*)dst, 16, 0, 0);
}
DI s16x4 tr_read(LAS char* p) { return __builtin_amdgcn_ds_read_tr16_b64_v4i16((LAS s16x4*)p); }
DI bf16x8 tr_frag(LAS char* p, int step) {
  const s16x4 lo = tr_read(p), hi = tr_read(p + step);
  return __builtin_bit_cast(bf16x8, __builtin_shufflevector(lo, hi, 0, 1, 2, 3, 4, 5, 6, 7));
}
DI f32x4 mfma16(bf16x8 a, bf16x8 b, f32x4 c) { return __builtin_amdgcn_mfma_f32_16x16x32_bf16(a, b, c, 0, 0, 0); }
DI float xsum_g(float v) {
  v += __shfl_xor(v, 16);
  v += __shfl_xor(v, 32);
  return v;
}
DI float gelu_scaled(float a, float k0r, float k1r, float ir) {
  const float e = __builtin_amdgcn_exp2f(a * __builtin_fmaf(a * a, k1r, k0r));
  return a * __builtin_amdgcn_rcpf(__builtin_fmaf(e, ir, ir));
}
constexpr float GK0 = -2.0f * 0.7978845608028654f * 1.4426950408889634f;
DI u32x4 swap_pair(u32x2 x, u32x2 y) {
  const auto a = __builtin_amdgcn_permlane16_swap(x.x, y.x, false, false);
  const auto b = __builtin_amdgcn_permlane16_swap(x.y, y.y, false, false);
  return (u32x4){a[0], b[0], a[1], b[1]};
}

template <int BN_, int WR_, int WC_, int NS_> struct Cfg {
  static constexpr int BN = BN_, WR = WR_, WC = WC_, NS = NS_;
  static constexpr int TM = 128 / WR, TN = BN / WC, MF = TM / 16, NF = TN / 16;
  static constexpr int ABYTES = 128 * 64 * 2, BBYTES = 64 * BN * 2, STAGE = ABYTES + BBYTES;
  static constexpr int NB = BN / 32, PER = 4 + NB, RB = BN * 2, CPR = BN / 8;
  static DI int swzB(int k) {
    return BN == 64 ? (((k >> 1) & 1) | (((k >> 3) & 1) << 1)) : BN == 128 ? ((2 * (k & 3)) | ((k >> 3) & 1)) : ((k >> 3) & 1);
  }
};

template <class C, class Tail, int J> DI void loader_tail_iters(LAS char* lds, int& buf, int lw, int lane, const Tail& tail) {
  if constexpr (J < C::NS - 1) {
    wait_vm<(C::NS - 2 - J) * C::PER + Tail::sum(J)>();
    BAR();
    tail.template issue<J>(lds + buf * C::STAGE, lw, lane);
    buf = (buf + 1 == C::NS) ? 0 : buf + 1;
    loader_tail_iters<C, Tail, J + 1>(lds, buf, lw, lane, tail);
  }
}

template <class C, class Tail>
DI void gemm_loader(LAS char* lds, const char* Ab, int lda_b, const char* Bb, int ldw_b, int nk, int lw, int lane, const Tail& tail) {
  const unsigned voffA = (unsigned)((8 * lw + (lane >> 3)) * lda_b + (((lane & 7) ^ ((4 * lw + (lane >> 4)) & 7)) << 4));
  unsigned voffB[C::NB];
#pragma unroll
  for (int i = 0; i < C::NB; ++i) {
    const int L = (lw + 4 * i) * 64 + lane, k = L / C::CPR, pos = L % C::CPR;
    voffB[i] = (unsigned)(k * ldw_b + ((pos ^ (C::swzB(k) << 1)) << 4));
  }
  auto issue = [&](int kt, int buf) {
    const char* a = Ab + kt * 128;
    const char* b = Bb + (size_t)kt * 64 * ldw_b;
    LAS char* dst = lds + buf * C::STAGE + lw * 1024;
#pragma unroll
    for (int i = 0; i < 4; ++i) glds16(a + (size_t)i * 32 * lda_b + voffA, dst + i * 4096);
#pragma unroll
    for (int i = 0; i < C::NB; ++i) glds16(b + voffB[i], dst + C::ABYTES + i * 4096);
  };
#pragma unroll
  for (int s = 0; s < C::NS - 1; ++s) issue(s, s);
  int buf = C::NS - 1;
  for (int kt = 0; kt < nk - (C::NS - 1); ++kt) {
    wait_vm<(C::NS - 2) * C::PER>();
    BAR();
    issue(kt + C::NS - 1, buf);
    buf = (buf + 1 == C::NS) ? 0 : buf + 1;
  }
  loader_tail_iters<C, Tail, 0>(lds, buf, lw, lane, tail);
  wait_vm<0>();
  BAR();
}
template <class C> DI int tail_buf(int nk, int j) { return (nk + j) % C::NS; }

template <int NM, int NL> DI void sched_mix() {
  if constexpr (NL <= NM) {
#pragma unroll
    for (int i = 0; i < NL; ++i) {
      __builtin_amdgcn_sched_group_barrier(0x8, 1, 0);
      __builtin_amdgcn_sched_group_barrier(0x100, 1, 0);
    }
    __builtin_amdgcn_sched_group_barrier(0x8, NM - NL, 0);
  } else {
#pragma unroll
    for (int i = 0; i < (NL + 1) / 2; ++i) {
      __builtin_amdgcn_sched_group_barrier(0x8, 1, 0);
      __builtin_amdgcn_sched_group_barrier(0x100, 2, 0);
    }
    __builtin_amdgcn_sched_group_barrier(0x8, NM - (NL + 1) / 2, 0);
  }
}

template <class C, bool STATS, class NfMap>
DI void gemm_consumer(LAS char* lds, int nk, int wr, int wc, int lane, f32x4 (&acc)[C::MF][C::NF], const NfMap& nfmap,
                      f32x4 (&rsum)[C::MF], f32x4 (&rsq)[C::MF], f32x4& csum) {
  const int g = lane >> 4, q = (lane & 15) >> 2, p = lane & 3;
  const int aoff0 = (wr * C::TM + (lane & 15)) * 128 + ((g ^ ((lane >> 1) & 7)) << 4);
  const int aoff1 = aoff0 ^ 64;
  int boff[C::NF];
#pragma unroll
  for (int f = 0; f < C::NF; ++f) {
    const int k = 8 * g + q;
    boff[f] = C::ABYTES + k * C::RB + (((wc * C::NF + nfmap(f)) ^ C::swzB(k)) << 5) + 8 * p;
  }
  bf16x8 a0[C::MF], b0[C::NF], a1[C::MF], b1[C::NF];
  const bf16x8 ones = __builtin_bit_cast(bf16x8, (u32x4){0x3f803f80u, 0x3f803f80u, 0x3f803f80u, 0x3f803f80u});
  auto ldfr = [&](bf16x8(&a)[C::MF], bf16x8(&b)[C::NF], LAS char* st, int ks) {
#pragma unroll
    for (int m = 0; m < C::MF; ++m) a[m] = *(LAS bf16x8*)(st + (ks ? aoff1 : aoff0) + m * 2048);
#pragma unroll
    for (int f = 0; f < C::NF; ++f) b[f] = tr_frag(st + boff[f] + ks * 32 * C::RB, 4 * C::RB);
  };
  auto mma = [&](bf16x8(&a)[C::MF], bf16x8(&b)[C::NF]) {
#pragma unroll
    for (int m = 0; m < C::MF; ++m)
#pragma unroll
      for (int f = 0; f < C::NF; ++f) acc[m][f] = mfma16(b[f], a[m], acc[m][f]);
    if constexpr (STATS) {
#pragma unroll
      for (int m = 0; m < C::MF; ++m) {
        rsum[m] = mfma16(ones, a[m], rsum[m]);
        rsq[m] = mfma16(a[m], a[m], rsq[m]);
      }
      csum = mfma16(b[0], ones, csum);
    }
  };
  constexpr int NM = C::MF * C::NF + (STATS ? 2 * C::MF + 1 : 0), NL = C::MF + 2 * C::NF;
  BAR();
  asm volatile("" ::: "memory");
  ldfr(a0, b0, lds, 0);
  int buf = 0;
  for (int kt = 0; kt < nk; ++kt) {
    ldfr(a1, b1, lds + buf * C::STAGE, 1);
    mma(a0, b0);
    sched_mix<NM, NL>();
    __builtin_amdgcn_sched_barrier(0);
    WAIT_LGKM0();
    BAR();
    asm volatile("" ::: "memory");
    __builtin_amdgcn_sched_barrier(0);
    buf = (buf + 1 == C::NS) ? 0 : buf + 1;
    ldfr(a0, b0, lds + buf * C::STAGE, 0);
    mma(a1, b1);
    sched_mix<NM, NL>();
    __builtin_amdgcn_sched_barrier(0);
  }
}

struct NoTail {
  static constexpr int sum(int) { return 0; }
  template <int J> DI void issue(LAS char*, int, int) const {}
};
template <int BN> DI int rt_swz(int row) { return BN == 64 ? ((row >> 1) & 7) : (row & 15); }
template <int BN> DI void load_row_tile(LAS char* dst, const char* src, int ld_b, int lw, int lane) {
  constexpr int CPR = BN / 8, N = 128 * CPR / 256;
#pragma unroll
  for (int i = 0; i < N; ++i) {
    const int L = (lw + 4 * i) * 64 + lane, row = L / CPR, pos = L % CPR;
    glds16(src + (size_t)row * ld_b + ((pos ^ rt_swz<BN>(row)) << 4), dst + (lw + 4 * i) * 1024);
  }
}
template <int BN> DI u32x2 read_row_tile(LAS char* t, int row, int col) {
  return *(LAS u32x2*)(t + row * (BN * 2) + (((col >> 3) ^ rt_swz<BN>(row)) << 4) + ((col & 4) << 1));
}

__global__ __launch_bounds__(256) void k_prep(const bf16_t* __restrict__ kk, const bf16_t* __restrict__ vv, const bf16_t* __restrict__ shk,
                                              bf16_t* __restrict__ uT) {
  const int wid = blockIdx.x * 4 + (threadIdx.x >> 6), lane = threadIdx.x & 63;
  const int t0 = (wid >> 4) * 64 + (lane >> 3) * 8, d0 = (wid & 15) * 64 + (lane & 7) * 8;
  u32x4 kr[11], vr[8], sk[4];
#pragma unroll
  for (int j = 0; j < 11; ++j) {
    const int t = t0 - 3 + j;
    kr[j] = *(const u32x4*)(kk + (size_t)(t < 0 ? 0 : t) * D + d0);
    if (t < 0) kr[j] = (u32x4){0u, 0u, 0u, 0u};
  }
#pragma unroll
  for (int j = 0; j < 8; ++j) vr[j] = *(const u32x4*)(vv + (size_t)(t0 + j) * D + d0);
#pragma unroll
  for (int j = 0; j < 4; ++j) sk[j] = *(const u32x4*)(shk + d0 * 4 + j * 8);
#pragma unroll
  for (int c = 0; c < 8; ++c) {
    const unsigned w0 = sk[c >> 1][(c & 1) * 2], w1 = sk[c >> 1][(c & 1) * 2 + 1];
    const float s0 = bf_lo(w0), s1 = bf_hi(w0), s2 = bf_lo(w1), s3 = bf_hi(w1);
    float kf[11], u[8];
#pragma unroll
    for (int j = 0; j < 11; ++j) kf[j] = (c & 1) ? bf_hi(kr[j][c >> 1]) : bf_lo(kr[j][c >> 1]);
#pragma unroll
    for (int j = 0; j < 8; ++j) {
      const float ks = kf[j] * s3 + kf[j + 1] * s2 + kf[j + 2] * s1 + kf[j + 3] * s0;
      u[j] = ks * ((c & 1) ? bf_hi(vr[j][c >> 1]) : bf_lo(vr[j][c >> 1]));
    }
    const u32x4 o = {pack_bf16(u[7], u[6]), pack_bf16(u[5], u[4]), pack_bf16(u[3], u[2]), pack_bf16(u[1], u[0])};
    *(u32x4*)(uT + (size_t)(d0 + c) * S + (S - 8 - t0)) = o;
  }
}

typedef Cfg<64, 4, 1, 6> C1;
constexpr int LDS1 = C1::NS * C1::STAGE + 256;
__global__ __launch_bounds__(512, 2) void k_g1(const bf16_t* __restrict__ x, const bf16_t* __restrict__ w1, bf16_t* __restrict__ hs,
                                               f32x2* __restrict__ stats1) {
  extern __shared__ __attribute__((aligned(16))) char smem[];
  LAS char* lds = (LAS char*)smem;
  const int wave = __builtin_amdgcn_readfirstlane(threadIdx.x >> 6), lane = threadIdx.x & 63;
  const int xcd = blockIdx.x & 7, idx = blockIdx.x >> 3;
  const int pm = (xcd >> 1) * 4 + (idx & 3), pn = (xcd & 1) * 8 + (idx >> 2);
  constexpr int nk = D / 64;
  if (wave >= 4) {
    gemm_loader<C1>(lds, (const char*)(x + (size_t)pm * 128 * D), D * 2, (const char*)(w1 + pn * 64), D * 2, nk, wave - 4, lane, NoTail());
    BAR();
    return;
  }
  const int wr = wave;
  f32x4 acc[2][4], rsum[2], rsq[2], csum = {0.f, 0.f, 0.f, 0.f};
#pragma unroll
  for (int m = 0; m < 2; ++m) {
    rsum[m] = rsq[m] = (f32x4){0.f, 0.f, 0.f, 0.f};
#pragma unroll
    for (int f = 0; f < 4; ++f) acc[m][f] = (f32x4){0.f, 0.f, 0.f, 0.f};
  }
  auto nfmap = [&](int f) { return f ^ wr; };
  gemm_consumer<C1, true>(lds, nk, wr, 0, lane, acc, nfmap, rsum, rsq, csum);
  const int g = lane >> 4, mm = lane & 15;
  LAS float* cs = (LAS float*)(lds + C1::NS * C1::STAGE);
  if (mm == 0) *(LAS f32x4*)(cs + wr * 16 + 4 * g) = csum;
  WAIT_LGKM0();
  BAR();
  asm volatile("" ::: "memory");
  f32x4 cw[4];
#pragma unroll
  for (int f = 0; f < 4; ++f) cw[f] = *(LAS f32x4*)(cs + (f ^ wr) * 16 + 4 * g);
  const int grp = pn >> 2, delta = (grp & 1) ? -((grp >> 1) + 1) : ((grp >> 1) + 1);
#pragma unroll
  for (int m = 0; m < 2; ++m) {
    const int sel = mm & 3;
    float dq = sel == 0 ? rsq[m][0] : sel == 1 ? rsq[m][1] : sel == 2 ? rsq[m][2] : rsq[m][3];
    dq = __shfl(dq, mm + 16 * (mm >> 2));
    const float mu = rsum[m][0] * (1.0f / D);
    const float var = fmaxf(dq * (1.0f / D) - mu * mu, 0.f);
    const float r = __builtin_amdgcn_rsqf(var + EPS);
    const float rr = r * __builtin_amdgcn_rsqf(var * r * r + EPS);
    const int row = pm * 128 + wr * 32 + m * 16 + mm;
    if (pn == 0 && g == 0) stats1[row] = (f32x2){mu, r};
    const float k0r = GK0 * rr, k1r = k0r * 0.044715f * rr * rr, ir = 1.0f / rr;
    u32x2 pk[4];
#pragma unroll
    for (int f = 0; f < 4; ++f) {
      float o[4];
#pragma unroll
      for (int j = 0; j < 4; ++j) o[j] = gelu_scaled(acc[m][f][j] - mu * cw[f][j], k0r, k1r, ir);
      pk[f] = (u32x2){pack_bf16(o[0], o[1]), pack_bf16(o[2], o[3])};
    }
    const int drow = row + delta;
#pragma unroll
    for (int f = 0; f < 4; f += 2) {
      const u32x4 o = swap_pair(pk[f], pk[f + 1]);
      const int col = pn * 64 + (((f + (g & 1)) ^ wr) << 4) + 8 * (g >> 1);
      if ((unsigned)drow < (unsigned)S) *(u32x4*)(hs + (size_t)drow * D + col) = o;
      else *(u32x4*)(hs + (size_t)(drow & (S - 1)) * D + col) = (u32x4){0u, 0u, 0u, 0u};
    }
  }
}

typedef Cfg<128, 2, 2, 4> C2;
struct Tail2 {
  const char* xs;
  static constexpr int sum(int j) { return j > 0 ? 8 : 0; }
  template <int J> DI void issue(LAS char* dst, int lw, int lane) const {
    if constexpr (J == 0) load_row_tile<128>(dst, xs, D * 2, lw, lane);
  }
};
constexpr int CV_K = 4608, CV_U = 4160, CV_W = CV_K + 2 * CV_U;
constexpr int LDS2 = C2::NS * C2::STAGE;
static_assert(8 * CV_W + 1024 <= LDS2, "conv LDS");

DI void conv_block(LAS char* lds, int cb, int wave, int lane, const bf16_t* __restrict__ uT, const bf16_t* __restrict__ ssk,
                   const bf16_t* __restrict__ ssd, const bf16_t* __restrict__ w3, bf16_t* __restrict__ yT, float* __restrict__ colsum3) {
  const int d = cb * 8 + wave;
  LAS char* base = lds + wave * CV_W;
  const char* ksrc = (const char*)(ssk + (size_t)d * S);
  const char* usrc = (const char*)(uT + (size_t)d * S);
  u32x4 sh[4];
#pragma unroll
  for (int i = 0; i < 4; ++i) sh[i] = *(const u32x4u*)(usrc + i * 1024 + lane * 16 + 2);
#pragma unroll
  for (int i = 0; i < 4; ++i) glds16(ksrc + i * 1024 + lane * 16, base + 512 + i * 1024);
#pragma unroll
  for (int i = 0; i < 4; ++i) glds16(usrc + i * 1024 + lane * 16, base + CV_K + i * 1024);
  u32x4 wv[8];
  const int cbc = cb < F / 32 ? cb : 0;
#pragma unroll
  for (int i = 0; i < 8; ++i) wv[i] = *(const u32x4*)(w3 + (size_t)(wave * 128 + i * 16 + (lane >> 2)) * F + cbc * 32 + (lane & 3) * 8);
  const float dcoef = bf_lo(ssd[d]);
  *(LAS u32x2*)(base + lane * 8) = (u32x2){0u, 0u};
  if (lane < 32) *(LAS unsigned*)(base + CV_K + 4096 + (lane >> 4) * CV_U + (lane & 15) * 4) = 0u;
  if (lane == 63) sh[3][3] &= 0xffffu;
#pragma unroll
  for (int i = 0; i < 4; ++i) *(LAS u32x4*)(base + CV_K + CV_U + i * 1024 + lane * 16) = sh[i];
  wait_vm<8>();
  WAIT_LGKM0();
  const int i16 = lane & 15, g = lane >> 4;
  LAS char* tb = base + CV_K + ((i16 & 1) ? 0 : CV_U - 2) + 2 * (2031 + 8 * g - i16) - 512 * 7;
  LAS char* rb = base + 480 + 32 * i16 + 16 * g;
  f32x4 acc[8];
#pragma unroll
  for (int j = 0; j < 8; ++j) acc[j] = (f32x4){0.f, 0.f, 0.f, 0.f};
#pragma unroll 2
  for (int r = 0; r < 8; ++r) {
    bf16x8 T[8], R[8];
#pragma unroll
    for (int a = 0; a < 8; ++a) {
      LAS unsigned* tp = (LAS unsigned*)(tb + 512 * (7 - a) - 64 * r);
      T[a] = __builtin_bit_cast(bf16x8, (u32x4){tp[0], tp[1], tp[2], tp[3]});
      R[a] = *(LAS bf16x8*)(rb + 512 * a - 64 * r);
    }
#pragma unroll
    for (int a = 0; a < 8; ++a)
#pragma unroll
      for (int e = 0; e + a < 8; ++e) acc[a + e] = mfma16(T[a], R[e], acc[a + e]);
  }
#pragma unroll
  for (int jb = 0; jb < 8; ++jb) {
    const int t = 256 * jb + 16 * i16 + 4 * g;
    const u32x2 uw = *(LAS u32x2*)(base + CV_K + 2 * (2044 - t));
    const float y0 = acc[jb][0] + bf_hi(uw.y) * dcoef, y1 = acc[jb][1] + bf_lo(uw.y) * dcoef;
    const float y2 = acc[jb][2] + bf_hi(uw.x) * dcoef, y3 = acc[jb][3] + bf_lo(uw.x) * dcoef;
    *(u32x2*)(yT + (size_t)d * S + t) = (u32x2){pack_bf16(y0, y1), pack_bf16(y2, y3)};
  }
  float cs[8];
#pragma unroll
  for (int c = 0; c < 8; ++c) {
    float s = 0.f;
#pragma unroll
    for (int i = 0; i < 8; ++i) s += (c & 1) ? bf_hi(wv[i][c >> 1]) : bf_lo(wv[i][c >> 1]);
    s += __shfl_xor(s, 4);
    s += __shfl_xor(s, 8);
    cs[c] = xsum_g(s);
  }
  LAS float* red = (LAS float*)(lds + 8 * CV_W);
  if (lane < 4) {
    *(LAS f32x4*)(red + wave * 32 + lane * 8) = (f32x4){cs[0], cs[1], cs[2], cs[3]};
    *(LAS f32x4*)(red + wave * 32 + lane * 8 + 4) = (f32x4){cs[4], cs[5], cs[6], cs[7]};
  }
  WAIT_LGKM0();
  BAR();
  asm volatile("" ::: "memory");
  if (wave == 0 && lane < 32 && cb < F / 32) {
    float s = 0.f;
#pragma unroll
    for (int w = 0; w < 8; ++w) s += red[w * 32 + lane];
    colsum3[cb * 32 + lane] = s;
  }
}

__global__ __launch_bounds__(512, 2) void k_g2(const bf16_t* __restrict__ hs, const bf16_t* __restrict__ w2, const bf16_t* __restrict__ x,
                                               const f32x2* __restrict__ stats1, bf16_t* __restrict__ x1, f32x2* __restrict__ stats2,
                                               const bf16_t* __restrict__ uT, const bf16_t* __restrict__ ssk, const bf16_t* __restrict__ ssd,
                                               const bf16_t* __restrict__ w3, bf16_t* __restrict__ yT, float* __restrict__ colsum3) {
  extern __shared__ __attribute__((aligned(16))) char smem[];
  LAS char* lds = (LAS char*)smem;
  const int wave = __builtin_amdgcn_readfirstlane(threadIdx.x >> 6), lane = threadIdx.x & 63;
  const int xcd = blockIdx.x & 7, idx = blockIdx.x >> 3;
  if (idx & 1) {
    conv_block(lds, xcd * 16 + (idx >> 1), wave, lane, uT, ssk, ssd, w3, yT, colsum3);
    return;
  }
  const int pm = (xcd >> 1) * 4 + ((idx >> 1) & 3), pn = (xcd & 1) * 4 + (idx >> 3);
  constexpr int nk = D / 64;
  if (wave >= 4) {
    Tail2 tl{(const char*)(x + (size_t)pm * 128 * D + pn * 128)};
    gemm_loader<C2>(lds, (const char*)(hs + (size_t)pm * 128 * D), D * 2, (const char*)(w2 + pn * 128), D * 2, nk, wave - 4, lane, tl);
    return;
  }
  const int wr = wave >> 1, wc = wave & 1, g = lane >> 4, mm = lane & 15;
  f32x2 st[4];
#pragma unroll
  for (int m = 0; m < 4; ++m) st[m] = stats1[pm * 128 + wr * 64 + m * 16 + mm];
  f32x4 acc[4][4], dum[4], dum1;
#pragma unroll
  for (int m = 0; m < 4; ++m)
#pragma unroll
    for (int f = 0; f < 4; ++f) acc[m][f] = (f32x4){0.f, 0.f, 0.f, 0.f};
  auto nfmap = [&](int f) { return f; };
  gemm_consumer<C2, false>(lds, nk, wr, wc, lane, acc, nfmap, dum, dum, dum1);
  LAS char* xt = lds + tail_buf<C2>(nk, 0) * C2::STAGE;
#pragma unroll
  for (int m = 0; m < 4; ++m) {
    const int rl = wr * 64 + m * 16 + mm, row = pm * 128 + rl;
    const float r = st[m].y, nmr = -st[m].x * r;
    u32x2 pk[4];
    float v[16], sum = 0.f;
#pragma unroll
    for (int f = 0; f < 4; ++f) {
      const u32x2 xw = read_row_tile<128>(xt, rl, wc * 64 + f * 16 + 4 * g);
      pk[f] = (u32x2){pack_bf16(acc[m][f][0] + (bf_lo(xw.x) * r + nmr), acc[m][f][1] + (bf_hi(xw.x) * r + nmr)),
                      pack_bf16(acc[m][f][2] + (bf_lo(xw.y) * r + nmr), acc[m][f][3] + (bf_hi(xw.y) * r + nmr))};
      v[4 * f] = bf_lo(pk[f].x), v[4 * f + 1] = bf_hi(pk[f].x), v[4 * f + 2] = bf_lo(pk[f].y), v[4 * f + 3] = bf_hi(pk[f].y);
      sum += (v[4 * f] + v[4 * f + 1]) + (v[4 * f + 2] + v[4 * f + 3]);
    }
#pragma unroll
    for (int f = 0; f < 4; f += 2) {
      const u32x4 o = swap_pair(pk[f], pk[f + 1]);
      *(u32x4*)(x1 + (size_t)row * D + pn * 128 + wc * 64 + ((f + (g & 1)) << 4) + 8 * (g >> 1)) = o;
    }
    const float mw = xsum_g(sum) * (1.0f / 64.0f);
    float m2 = 0.f;
#pragma unroll
    for (int j = 0; j < 16; ++j) m2 += (v[j] - mw) * (v[j] - mw);
    m2 = xsum_g(m2);
    if (g == 0) stats2[row * 16 + pn * 2 + wc] = (f32x2){mw, m2};
  }
}

typedef Cfg<224, 2, 2, 3> C3;
constexpr int LDS3 = C3::NS * C3::STAGE;
__global__ __launch_bounds__(512, 2) void k_g3(const bf16_t* __restrict__ x1, const bf16_t* __restrict__ w3, const f32x2* __restrict__ stats2,
                                               const float* __restrict__ colsum3, bf16_t* __restrict__ h3) {
  extern __shared__ __attribute__((aligned(16))) char smem[];
  LAS char* lds = (LAS char*)smem;
  const int wave = __builtin_amdgcn_readfirstlane(threadIdx.x >> 6), lane = threadIdx.x & 63;
  const int xcd = blockIdx.x & 7, idx = blockIdx.x >> 3;
  const int pm = (xcd >> 2) * 8 + (idx & 7), pn = (xcd & 3) * 4 + (idx >> 3);
  constexpr int nk = D / 64;
  if (wave >= 4) {
    gemm_loader<C3>(lds, (const char*)(x1 + (size_t)pm * 128 * D), D * 2, (const char*)(w3 + pn * 224), F * 2, nk, wave - 4, lane, NoTail());
    return;
  }
  const int wr = wave >> 1, wc = wave & 1, g = lane >> 4, mm = lane & 15;
  auto nfmap = [&](int f) { return f + wc == 7 ? 0 : f + wc; };
  float rs[4];
  f32x4 acc[4][7], dum[4], dum1;
  {
    f32x4 cw[7];
#pragma unroll
    for (int f = 0; f < 7; ++f) cw[f] = *(const f32x4*)(colsum3 + pn * 224 + wc * 112 + nfmap(f) * 16 + 4 * g);
#pragma unroll
    for (int m = 0; m < 4; ++m) {
      const f32x4* sp = (const f32x4*)(stats2 + (pm * 128 + wr * 64 + m * 16 + mm) * 16 + 4 * g);
      const f32x4 p0 = sp[0], p1 = sp[1];
      const float mu = xsum_g((p0[0] + p0[2]) + (p1[0] + p1[2])) * (1.0f / 16.0f);
      const float d0 = p0[0] - mu, d1 = p0[2] - mu, d2 = p1[0] - mu, d3 = p1[2] - mu;
      const float m2 = xsum_g((p0[1] + p0[3]) + (p1[1] + p1[3]) + 64.0f * ((d0 * d0 + d1 * d1) + (d2 * d2 + d3 * d3)));
      rs[m] = __builtin_amdgcn_rsqf(m2 * (1.0f / D) + EPS);
#pragma unroll
      for (int f = 0; f < 7; ++f) acc[m][f] = cw[f] * (-mu);
    }
  }
  gemm_consumer<C3, false>(lds, nk, wr, wc, lane, acc, nfmap, dum, dum, dum1);
  u32x2 last[4];
#pragma unroll
  for (int m = 0; m < 4; ++m) {
    const int row = pm * 128 + wr * 64 + m * 16 + mm;
    const float k0r = GK0 * rs[m], k1r = k0r * 0.044715f * rs[m] * rs[m], ir = 1.0f / rs[m];
    u32x2 pk[7];
#pragma unroll
    for (int f = 0; f < 7; ++f)
      pk[f] = (u32x2){pack_bf16(gelu_scaled(acc[m][f][0], k0r, k1r, ir), gelu_scaled(acc[m][f][1], k0r, k1r, ir)),
                      pack_bf16(gelu_scaled(acc[m][f][2], k0r, k1r, ir), gelu_scaled(acc[m][f][3], k0r, k1r, ir))};
#pragma unroll
    for (int f = 0; f < 6; f += 2) {
      const u32x4 o = swap_pair(pk[f], pk[f + 1]);
      *(u32x4*)(h3 + (size_t)row * F + pn * 224 + wc * 128 + ((f + (g & 1)) << 4) + 8 * (g >> 1)) = o;
    }
    last[m] = pk[6];
    if (m & 1) {
      const u32x4 o = swap_pair(last[m - 1], last[m]);
      *(u32x4*)(h3 + (size_t)(row - 16 + 16 * (g & 1)) * F + pn * 224 + 96 + wc * 16 + 8 * (g >> 1)) = o;
    }
  }
}

typedef Cfg<64, 4, 1, 6> C4;
constexpr int LDS4 = C4::NS * C4::STAGE;
struct Tail4 {
  const char* xs;
  const char* x1s;
  const char* ys;
  static constexpr int sum(int j) { return 4 * (j < 3 ? j : 3); }
  template <int J> DI void issue(LAS char* dst, int lw, int lane) const {
    if constexpr (J == 0) load_row_tile<64>(dst, xs, D * 2, lw, lane);
    if constexpr (J == 1) load_row_tile<64>(dst, x1s, D * 2, lw, lane);
    if constexpr (J == 2) {
#pragma unroll
      for (int i = 0; i < 4; ++i) {
        const int row = (lw + 4 * i) * 4 + (lane >> 4), pos = lane & 15;
        glds16(ys + (size_t)row * (S * 2) + ((pos ^ ((row & 7) << 1)) << 4), dst + (lw + 4 * i) * 1024);
      }
    }
  }
};
__global__ __launch_bounds__(512, 2) void k_g4(const bf16_t* __restrict__ h3, const bf16_t* __restrict__ w4, const bf16_t* __restrict__ x,
                                               const bf16_t* __restrict__ x1, const bf16_t* __restrict__ yT, const f32x2* __restrict__ stats1,
                                               bf16_t* __restrict__ out) {
  extern __shared__ __attribute__((aligned(16))) char smem[];
  LAS char* lds = (LAS char*)smem;
  const int wave = __builtin_amdgcn_readfirstlane(threadIdx.x >> 6), lane = threadIdx.x & 63;
  const int xcd = blockIdx.x & 7, idx = blockIdx.x >> 3;
  const int pm = (xcd >> 1) * 4 + (idx & 3), pn = (xcd & 1) * 8 + (idx >> 2);
  constexpr int nk = F / 64;
  if (wave >= 4) {
    Tail4 tl{(const char*)(x + (size_t)pm * 128 * D + pn * 64), (const char*)(x1 + (size_t)pm * 128 * D + pn * 64),
             (const char*)(yT + (size_t)pn * 64 * S + pm * 128)};
    gemm_loader<C4>(lds, (const char*)(h3 + (size_t)pm * 128 * F), F * 2, (const char*)(w4 + pn * 64), D * 2, nk, wave - 4, lane, tl);
    return;
  }
  const int wr = wave, g = lane >> 4, mm = lane & 15;
  f32x2 st[2];
#pragma unroll
  for (int m = 0; m < 2; ++m) st[m] = stats1[pm * 128 + wr * 32 + m * 16 + mm];
  f32x4 acc[2][4], dum[2], dum1;
#pragma unroll
  for (int m = 0; m < 2; ++m)
#pragma unroll
    for (int f = 0; f < 4; ++f) acc[m][f] = (f32x4){0.f, 0.f, 0.f, 0.f};
  auto nfmap = [&](int f) { return f; };
  gemm_consumer<C4, false>(lds, nk, wr, 0, lane, acc, nfmap, dum, dum, dum1);
  LAS char* xt = lds + tail_buf<C4>(nk, 0) * C4::STAGE;
  LAS char* x1t = lds + tail_buf<C4>(nk, 1) * C4::STAGE;
  LAS char* yt = lds + tail_buf<C4>(nk, 2) * C4::STAGE;
#pragma unroll
  for (int m = 0; m < 2; ++m) {
    const int rl = wr * 32 + m * 16 + mm, row = pm * 128 + rl;
    const float r = st[m].y, nmr = -st[m].x * r;
    u32x2 pk[4];
#pragma unroll
    for (int f = 0; f < 4; ++f) {
      const u32x2 xw = read_row_tile<64>(xt, rl, f * 16 + 4 * g);
      const u32x2 x1w = read_row_tile<64>(x1t, rl, f * 16 + 4 * g);
      const int yrow = f * 16 + 4 * g + (mm >> 2);
      const u32x2 yw = __builtin_bit_cast(u32x2, tr_read(yt + yrow * 256 + (((wr * 2 + m) ^ (yrow & 7)) << 5) + 8 * (mm & 3)));
      const float xv[4] = {bf_lo(xw.x), bf_hi(xw.x), bf_lo(xw.y), bf_hi(xw.y)};
      const float x1v[4] = {bf_lo(x1w.x), bf_hi(x1w.x), bf_lo(x1w.y), bf_hi(x1w.y)};
      const float yv[4] = {bf_lo(yw.x), bf_hi(yw.x), bf_lo(yw.y), bf_hi(yw.y)};
      float o[4];
#pragma unroll
      for (int j = 0; j < 4; ++j) {
        const float z = x1v[j] + acc[m][f][j];
        const float gt = z * __builtin_amdgcn_rcpf(1.0f + __builtin_amdgcn_exp2f(z * -1.4426950408889634f));
        o[j] = xv[j] + (xv[j] * r + nmr) * yv[j] * gt;
      }
      pk[f] = (u32x2){pack_bf16(o[0], o[1]), pack_bf16(o[2], o[3])};
    }
#pragma unroll
    for (int f = 0; f < 4; f += 2) {
      const u32x4 o = swap_pair(pk[f], pk[f + 1]);
      *(u32x4*)(out + (size_t)row * D + pn * 64 + ((f + (g & 1)) << 4) + 8 * (g >> 1)) = o;
    }
  }
}

extern "C" void kernel_launch(void* const* d_in, const int* in_sizes, int n_in, void* d_out, int out_size, void* d_ws, size_t ws_size,
                              hipStream_t stream) {
  const bf16_t* x = (const bf16_t*)d_in[0];
  const bf16_t* kk = (const bf16_t*)d_in[1];
  const bf16_t* vv = (const bf16_t*)d_in[2];
  const bf16_t* shk = (const bf16_t*)d_in[3];
  const bf16_t* ssk = (const bf16_t*)d_in[4];
  const bf16_t* ssd = (const bf16_t*)d_in[5];
  const bf16_t* w1 = (const bf16_t*)d_in[7];
  const bf16_t* w2 = (const bf16_t*)d_in[8];
  const bf16_t* w3 = (const bf16_t*)d_in[9];
  const bf16_t* w4 = (const bf16_t*)d_in[10];
  char* ws = (char*)d_ws;
  constexpr size_t SD = (size_t)S * D * 2;
  bf16_t* uT = (bf16_t*)(ws);
  bf16_t* hs = (bf16_t*)(ws + SD);
  bf16_t* yT = (bf16_t*)(ws + 2 * SD);
  bf16_t* x1 = (bf16_t*)(ws + 3 * SD);
  bf16_t* h3 = (bf16_t*)(ws + 4 * SD);
  char* tail = ws + 4 * SD + (size_t)S * F * 2;
  f32x2* stats1 = (f32x2*)tail;
  f32x2* stats2 = (f32x2*)(tail + S * 8);
  float* colsum3 = (float*)(tail + S * 8 + S * 128);
  (void)hipFuncSetAttribute((const void*)k_g1, hipFuncAttributeMaxDynamicSharedMemorySize, LDS1);
  (void)hipFuncSetAttribute((const void*)k_g2, hipFuncAttributeMaxDynamicSharedMemorySize, LDS2);
  (void)hipFuncSetAttribute((const void*)k_g3, hipFuncAttributeMaxDynamicSharedMemorySize, LDS3);
  (void)hipFuncSetAttribute((const void*)k_g4, hipFuncAttributeMaxDynamicSharedMemorySize, LDS4);
  k_prep<<<128, 256, 0, stream>>>(kk, vv, shk, uT);
  k_g1<<<256, 512, LDS1, stream>>>(x, w1, hs, stats1);
  k_g2<<<256, 512, LDS2, stream>>>(hs, w2, x, stats1, x1, stats2, uT, ssk, ssd, w3, yT, colsum3);
  k_g3<<<256, 512, LDS3, stream>>>(x1, w3, stats2, colsum3, h3);
  k_g4<<<256, 512, LDS4, stream>>>(h3, w4, x, x1, yT, stats1, (bf16_t*)d_out);
}
```

```cpp
#include <hip/hip_runtime.h>
#include <stdint.h>

#define LAS __attribute__((address_space(3)))
#define DI __device__ __forceinline__
typedef unsigned short bf16_t;
typedef __bf16 bf16x8 __attribute__((ext_vector_type(8)));
typedef __bf16 bf16x2 __attribute__((ext_vector_type(2)));
typedef short s16x4 __attribute__((ext_vector_type(4)));
typedef short s16x8 __attribute__((ext_vector_type(8)));
typedef float f32x2 __attribute__((ext_vector_type(2)));
typedef float f32x4 __attribute__((ext_vector_type(4)));
typedef unsigned u32x2 __attribute__((ext_vector_type(2)));
typedef unsigned u32x4 __attribute__((ext_vector_type(4)));
typedef unsigned u32x4u __attribute__((ext_vector_type(4), aligned(2)));

constexpr int S = 2048, D = 1024, F = 3584;
constexpr float EPS = 1e-5f;

#define PROF_ARG
#define PROF_PASS
#define PROF_DECL
#define STAMP(i)
#define PROF_END(kid)
DI float bf_lo(unsigned w) { return __uint_as_float(w << 16); }
DI float bf_hi(unsigned w) { return __uint_as_float(w & 0xffff0000u); }
DI unsigned pack_bf16(float lo, float hi) {
  f32x2 v = {lo, hi};
  return __builtin_bit_cast(unsigned, __builtin_convertvector(v, bf16x2));
}
#define BAR() __builtin_amdgcn_s_barrier()
#define WAIT_LGKM0() asm volatile("s_waitcnt lgkmcnt(0)" ::: "memory")
template <int N> DI void wait_vm() { asm volatile("s_waitcnt vmcnt(%0)" ::"n"(N) : "memory"); }
DI void glds16(const void* src, LAS char* dst) {
  __builtin_amdgcn_global_load_lds((const unsigned*)src, (LAS unsigned*)dst, 16, 0, 0);
}
DI s16x4 tr_read(LAS char* p) { return __builtin_amdgcn_ds_read_tr16_b64_v4i16((LAS s16x4*)p); }
DI bf16x8 tr_frag(LAS char* p, int step) {
  const s16x4 lo = tr_read(p), hi = tr_read(p + step);
  return __builtin_bit_cast(bf16x8, __builtin_shufflevector(lo, hi, 0, 1, 2, 3, 4, 5, 6, 7));
}
DI f32x4 mfma16(bf16x8 a, bf16x8 b, f32x4 c) { return __builtin_amdgcn_mfma_f32_16x16x32_bf16(a, b, c, 0, 0, 0); }
DI float xsum_g(float v) {
  v += __shfl_xor(v, 16);
  v += __shfl_xor(v, 32);
  return v;
}
DI float gelu_scaled(float a, float k0r, float k1r, float ir) {
  const float e = __builtin_amdgcn_exp2f(a * __builtin_fmaf(a * a, k1r, k0r));
  return a * __builtin_amdgcn_rcpf(__builtin_fmaf(e, ir, ir));
}
constexpr float GK0 = -2.0f * 0.7978845608028654f * 1.4426950408889634f;
DI u32x4 swap_pair(u32x2 x, u32x2 y) {
  const auto a = __builtin_amdgcn_permlane16_swap(x.x, y.x, false, false);
  const auto b = __builtin_amdgcn_permlane16_swap(x.y, y.y, false, false);
  return (u32x4){a[0], b[0], a[1], b[1]};
}

template <int BN_, int WR_, int WC_, int NS_> struct Cfg {
  static constexpr int BN = BN_, WR = WR_, WC = WC_, NS = NS_;
  static constexpr int TM = 128 / WR, TN = BN / WC, MF = TM / 16, NF = TN / 16;
  static constexpr int ABYTES = 128 * 64 * 2, BBYTES = 64 * BN * 2, STAGE = ABYTES + BBYTES;
  static constexpr int NB = BN / 32, PER = 4 + NB, RB = BN * 2, CPR = BN / 8;
  static DI int swzB(int k) {
    return BN == 64 ? (((k >> 1) & 1) | (((k >> 3) & 1) << 1)) : BN == 128 ? ((2 * (k & 3)) | ((k >> 3) & 1)) : ((k >> 3) & 1);
  }
};

template <class C, class Tail, int J> DI void loader_tail_iters(LAS char* lds, int& buf, int lw, int lane, const Tail& tail) {
  if constexpr (J < C::NS - 1) {
    wait_vm<(C::NS - 2 - J) * C::PER + Tail::sum(J)>();
    BAR();
    tail.template issue<J>(lds + buf * C::STAGE, lw, lane);
    buf = (buf + 1 == C::NS) ? 0 : buf + 1;
    loader_tail_iters<C, Tail, J + 1>(lds, buf, lw, lane, tail);
  }
}

template <class C, class Tail>
DI void gemm_loader(LAS char* lds, const char* Ab, int lda_b, const char* Bb, int ldw_b, int nk, int lw, int lane, const Tail& tail PROF_ARG) {
  const unsigned voffA = (unsigned)((8 * lw + (lane >> 3)) * lda_b + (((lane & 7) ^ ((4 * lw + (lane >> 4)) & 7)) << 4));
  unsigned voffB[C::NB];
#pragma unroll
  for (int i = 0; i < C::NB; ++i) {
    const int L = (lw + 4 * i) * 64 + lane, k = L / C::CPR, pos = L % C::CPR;
    voffB[i] = (unsigned)(k * ldw_b + ((pos ^ (C::swzB(k) << 1)) << 4));
  }
  auto issue = [&](int kt, int buf) {
    const char* a = Ab + kt * 128;
    const char* b = Bb + (size_t)kt * 64 * ldw_b;
    LAS char* dst = lds + buf * C::STAGE + lw * 1024;
#pragma unroll
    for (int i = 0; i < 4; ++i) glds16(a + (size_t)i * 32 * lda_b + voffA, dst + i * 4096);
#pragma unroll
    for (int i = 0; i < C::NB; ++i) glds16(b + voffB[i], dst + C::ABYTES + i * 4096);
  };
#pragma unroll
  for (int s = 0; s < C::NS - 1; ++s) issue(s, s);
  int buf = C::NS - 1;
  STAMP(1);
  for (int kt = 0; kt < nk - (C::NS - 1); ++kt) {
    wait_vm<(C::NS - 2) * C::PER>();
    BAR();
    issue(kt + C::NS - 1, buf);
    buf = (buf + 1 == C::NS) ? 0 : buf + 1;
  }
  STAMP(2);
  loader_tail_iters<C, Tail, 0>(lds, buf, lw, lane, tail);
  wait_vm<0>();
  BAR();
}
template <class C> DI int tail_buf(int nk, int j) { return (nk + j) % C::NS; }

template <int NM, int NL> DI void sched_mix() {
  if constexpr (NL <= NM) {
#pragma unroll
    for (int i = 0; i < NL; ++i) {
      __builtin_amdgcn_sched_group_barrier(0x8, 1, 0);
      __builtin_amdgcn_sched_group_barrier(0x100, 1, 0);
    }
    __builtin_amdgcn_sched_group_barrier(0x8, NM - NL, 0);
  } else {
#pragma unroll
    for (int i = 0; i < (NL + 1) / 2; ++i) {
      __builtin_amdgcn_sched_group_barrier(0x8, 1, 0);
      __builtin_amdgcn_sched_group_barrier(0x100, 2, 0);
    }
    __builtin_amdgcn_sched_group_barrier(0x8, NM - (NL + 1) / 2, 0);
  }
}

template <class C, bool STATS, class NfMap>
DI void gemm_consumer(LAS char* lds, int nk, int wr, int wc, int lane, f32x4 (&acc)[C::MF][C::NF], const NfMap& nfmap,
                      f32x4 (&rsum)[C::MF], f32x4 (&rsq)[C::MF], f32x4& csum PROF_ARG) {
  const int g = lane >> 4, q = (lane & 15) >> 2, p = lane & 3;
  const int aoff0 = (wr * C::TM + (lane & 15)) * 128 + ((g ^ ((lane >> 1) & 7)) << 4);
  const int aoff1 = aoff0 ^ 64;
  int boff[C::NF];
#pragma unroll
  for (int f = 0; f < C::NF; ++f) {
    const int k = 8 * g + q;
    boff[f] = C::ABYTES + k * C::RB + (((wc * C::NF + nfmap(f)) ^ C::swzB(k)) << 5) + 8 * p;
  }
  bf16x8 a0[C::MF], b0[C::NF], a1[C::MF], b1[C::NF];
  const bf16x8 ones = __builtin_bit_cast(bf16x8, (u32x4){0x3f803f80u, 0x3f803f80u, 0x3f803f80u, 0x3f803f80u});
  auto ldfr = [&](bf16x8(&a)[C::MF], bf16x8(&b)[C::NF], LAS char* st, int ks) {
#pragma unroll
    for (int m = 0; m < C::MF; ++m) a[m] = *(LAS bf16x8*)(st + (ks ? aoff1 : aoff0) + m * 2048);
#pragma unroll
    for (int f = 0; f < C::NF; ++f) b[f] = tr_frag(st + boff[f] + ks * 32 * C::RB, 4 * C::RB);
  };
  auto mma = [&](bf16x8(&a)[C::MF], bf16x8(&b)[C::NF]) {
#pragma unroll
    for (int m = 0; m < C::MF; ++m)
#pragma unroll
      for (int f = 0; f < C::NF; ++f) acc[m][f] = mfma16(b[f], a[m], acc[m][f]);
    if constexpr (STATS) {
#pragma unroll
      for (int m = 0; m < C::MF; ++m) {
        rsum[m] = mfma16(ones, a[m], rsum[m]);
        rsq[m] = mfma16(a[m], a[m], rsq[m]);
      }
      csum = mfma16(b[0], ones, csum);
    }
  };
  constexpr int NM = C::MF * C::NF + (STATS ? 2 * C::MF + 1 : 0), NL = C::MF + 2 * C::NF;
  STAMP(1);
  BAR();
  asm volatile("" ::: "memory");
  STAMP(2);
  ldfr(a0, b0, lds, 0);
  int buf = 0;
  for (int kt = 0; kt < nk; ++kt) {
    ldfr(a1, b1, lds + buf * C::STAGE, 1);
    mma(a0, b0);
    sched_mix<NM, NL>();
    __builtin_amdgcn_sched_barrier(0);
    WAIT_LGKM0();
    BAR();
    asm volatile("" ::: "memory");
    __builtin_amdgcn_sched_barrier(0);
    buf = (buf + 1 == C::NS) ? 0 : buf + 1;
    ldfr(a0, b0, lds + buf * C::STAGE, 0);
    mma(a1, b1);
    sched_mix<NM, NL>();
    __builtin_amdgcn_sched_barrier(0);
  }
  STAMP(3);
}

struct NoTail {
  static constexpr int sum(int) { return 0; }
  template <int J> DI void issue(LAS char*, int, int) const {}
};
template <int BN> DI int rt_swz(int row) { return BN == 64 ? ((row >> 1) & 7) : (row & 15); }
template <int BN> DI void load_row_tile(LAS char* dst, const char* src, int ld_b, int lw, int lane) {
  constexpr int CPR = BN / 8, N = 128 * CPR / 256;
#pragma unroll
  for (int i = 0; i < N; ++i) {
    const int L = (lw + 4 * i) * 64 + lane, row = L / CPR, pos = L % CPR;
    glds16(src + (size_t)row * ld_b + ((pos ^ rt_swz<BN>(row)) << 4), dst + (lw + 4 * i) * 1024);
  }
}
template <int BN> DI u32x2 read_row_tile(LAS char* t, int row, int col) {
  return *(LAS u32x2*)(t + row * (BN * 2) + (((col >> 3) ^ rt_swz<BN>(row)) << 4) + ((col & 4) << 1));
}

__global__ __launch_bounds__(256) void k_prep(const bf16_t* __restrict__ kk, const bf16_t* __restrict__ vv, const bf16_t* __restrict__ shk,
                                              bf16_t* __restrict__ uT) {
  PROF_DECL;
  const int wid = blockIdx.x * 4 + (threadIdx.x >> 6), lane = threadIdx.x & 63;
  const int t0 = (wid >> 4) * 64 + (lane >> 3) * 8, d0 = (wid & 15) * 64 + (lane & 7) * 8;
  u32x4 kr[11], vr[8], sk[4];
#pragma unroll
  for (int j = 0; j < 11; ++j) {
    const int t = t0 - 3 + j;
    kr[j] = *(const u32x4*)(kk + (size_t)(t < 0 ? 0 : t) * D + d0);
    if (t < 0) kr[j] = (u32x4){0u, 0u, 0u, 0u};
  }
#pragma unroll
  for (int j = 0; j < 8; ++j) vr[j] = *(const u32x4*)(vv + (size_t)(t0 + j) * D + d0);
#pragma unroll
  for (int j = 0; j < 4; ++j) sk[j] = *(const u32x4*)(shk + d0 * 4 + j * 8);
#pragma unroll
  for (int c = 0; c < 8; ++c) {
    const unsigned w0 = sk[c >> 1][(c & 1) * 2], w1 = sk[c >> 1][(c & 1) * 2 + 1];
    const float s0 = bf_lo(w0), s1 = bf_hi(w0), s2 = bf_lo(w1), s3 = bf_hi(w1);
    float kf[11], u[8];
#pragma unroll
    for (int j = 0; j < 11; ++j) kf[j] = (c & 1) ? bf_hi(kr[j][c >> 1]) : bf_lo(kr[j][c >> 1]);
#pragma unroll
    for (int j = 0; j < 8; ++j) {
      const float ks = kf[j] * s3 + kf[j + 1] * s2 + kf[j + 2] * s1 + kf[j + 3] * s0;
      u[j] = ks * ((c & 1) ? bf_hi(vr[j][c >> 1]) : bf_lo(vr[j][c >> 1]));
    }
    const u32x4 o = {pack_bf16(u[7], u[6]), pack_bf16(u[5], u[4]), pack_bf16(u[3], u[2]), pack_bf16(u[1], u[0])};
    *(u32x4*)(uT + (size_t)(d0 + c) * S + (S - 8 - t0)) = o;
  }
  PROF_END(0);
}

typedef Cfg<64, 4, 1, 6> C1;
constexpr int LDS1 = C1::NS * C1::STAGE + 256;
__global__ __launch_bounds__(512, 2) void k_g1(const bf16_t* __restrict__ x, const bf16_t* __restrict__ w1, bf16_t* __restrict__ hs,
                                               f32x2* __restrict__ stats1) {
  extern __shared__ __attribute__((aligned(16))) char smem[];
  LAS char* lds = (LAS char*)smem;
  PROF_DECL;
  const int wave = __builtin_amdgcn_readfirstlane(threadIdx.x >> 6), lane = threadIdx.x & 63;
  const int xcd = blockIdx.x & 7, idx = blockIdx.x >> 3;
  const int pm = (xcd >> 1) * 4 + (idx & 3), pn = (xcd & 1) * 8 + (idx >> 2);
  constexpr int nk = D / 64;
  if (wave >= 4) {
    gemm_loader<C1>(lds, (const char*)(x + (size_t)pm * 128 * D), D * 2, (const char*)(w1 + pn * 64), D * 2, nk, wave - 4, lane, NoTail() PROF_PASS);
    BAR();
    PROF_END(1);
    return;
  }
  const int wr = wave;
  f32x4 acc[2][4], rsum[2], rsq[2], csum = {0.f, 0.f, 0.f, 0.f};
#pragma unroll
  for (int m = 0; m < 2; ++m) {
    rsum[m] = rsq[m] = (f32x4){0.f, 0.f, 0.f, 0.f};
#pragma unroll
    for (int f = 0; f < 4; ++f) acc[m][f] = (f32x4){0.f, 0.f, 0.f, 0.f};
  }
  auto nfmap = [&](int f) { return f ^ wr; };
  gemm_consumer<C1, true>(lds, nk, wr, 0, lane, acc, nfmap, rsum, rsq, csum PROF_PASS);
  const int g = lane >> 4, mm = lane & 15;
  LAS float* cs = (LAS float*)(lds + C1::NS * C1::STAGE);
  if (mm == 0) *(LAS f32x4*)(cs + wr * 16 + 4 * g) = csum;
  WAIT_LGKM0();
  BAR();
  asm volatile("" ::: "memory");
  f32x4 cw[4];
#pragma unroll
  for (int f = 0; f < 4; ++f) cw[f] = *(LAS f32x4*)(cs + (f ^ wr) * 16 + 4 * g);
  const int grp = pn >> 2, delta = (grp & 1) ? -((grp >> 1) + 1) : ((grp >> 1) + 1);
#pragma unroll
  for (int m = 0; m < 2; ++m) {
    const int sel = mm & 3;
    float dq = sel == 0 ? rsq[m][0] : sel == 1 ? rsq[m][1] : sel == 2 ? rsq[m][2] : rsq[m][3];
    dq = __shfl(dq, mm + 16 * (mm >> 2));
    const float mu = rsum[m][0] * (1.0f / D);
    const float var = fmaxf(dq * (1.0f / D) - mu * mu, 0.f);
    const float r = __builtin_amdgcn_rsqf(var + EPS);
    const float rr = r * __builtin_amdgcn_rsqf(var * r * r + EPS);
    const int row = pm * 128 + wr * 32 + m * 16 + mm;
    if (pn == 0 && g == 0) stats1[row] = (f32x2){mu, r};
    const float k0r = GK0 * rr, k1r = k0r * 0.044715f * rr * rr, ir = 1.0f / rr;
    u32x2 pk[4];
#pragma unroll
    for (int f = 0; f < 4; ++f) {
      float o[4];
#pragma unroll
      for (int j = 0; j < 4; ++j) o[j] = gelu_scaled(acc[m][f][j] - mu * cw[f][j], k0r, k1r, ir);
      pk[f] = (u32x2){pack_bf16(o[0], o[1]), pack_bf16(o[2], o[3])};
    }
    const int drow = row + delta;
#pragma unroll
    for (int f = 0; f < 4; f += 2) {
      const u32x4 o = swap_pair(pk[f], pk[f + 1]);
      const int col = pn * 64 + (((f + (g & 1)) ^ wr) << 4) + 8 * (g >> 1);
      if ((unsigned)drow < (unsigned)S) *(u32x4*)(hs + (size_t)drow * D + col) = o;
      else *(u32x4*)(hs + (size_t)(drow & (S - 1)) * D + col) = (u32x4){0u, 0u, 0u, 0u};
    }
  }
  PROF_END(1);
}

typedef Cfg<128, 2, 2, 4> C2;
struct Tail2 {
  const char* xs;
  static constexpr int sum(int j) { return j > 0 ? 8 : 0; }
  template <int J> DI void issue(LAS char* dst, int lw, int lane) const {
    if constexpr (J == 0) load_row_tile<128>(dst, xs, D * 2, lw, lane);
  }
};
constexpr int CV_K = 4608, CV_U = 4160, CV_W = CV_K + 2 * CV_U;
constexpr int LDS2 = C2::NS * C2::STAGE;
static_assert(8 * CV_W + 1024 <= LDS2, "conv LDS");

DI void conv_block(LAS char* lds, int cb, int wave, int lane, const bf16_t* __restrict__ uT, const bf16_t* __restrict__ ssk,
                   const bf16_t* __restrict__ ssd, const bf16_t* __restrict__ w3, bf16_t* __restrict__ yT, float* __restrict__ colsum3 PROF_ARG) {
  const int d = cb * 8 + wave;
  LAS char* base = lds + wave * CV_W;
  const char* ksrc = (const char*)(ssk + (size_t)d * S);
  const char* usrc = (const char*)(uT + (size_t)d * S);
  u32x4 sh[4];
#pragma unroll
  for (int i = 0; i < 4; ++i) sh[i] = *(const u32x4u*)(usrc + i * 1024 + lane * 16 + 2);
#pragma unroll
  for (int i = 0; i < 4; ++i) glds16(ksrc + i * 1024 + lane * 16, base + 512 + i * 1024);
#pragma unroll
  for (int i = 0; i < 4; ++i) glds16(usrc + i * 1024 + lane * 16, base + CV_K + i * 1024);
  u32x4 wv[8];
  const int cbc = cb < F / 32 ? cb : 0;
#pragma unroll
  for (int i = 0; i < 8; ++i) wv[i] = *(const u32x4*)(w3 + (size_t)(wave * 128 + i * 16 + (lane >> 2)) * F + cbc * 32 + (lane & 3) * 8);
  const float dcoef = bf_lo(ssd[d]);
  *(LAS u32x2*)(base + lane * 8) = (u32x2){0u, 0u};
  if (lane < 32) *(LAS unsigned*)(base + CV_K + 4096 + (lane >> 4) * CV_U + (lane & 15) * 4) = 0u;
  if (lane == 63) sh[3][3] &= 0xffffu;
#pragma unroll
  for (int i = 0; i < 4; ++i) *(LAS u32x4*)(base + CV_K + CV_U + i * 1024 + lane * 16) = sh[i];
  wait_vm<8>();
  WAIT_LGKM0();
  STAMP(2);
  const int i16 = lane & 15, g = lane >> 4;
  LAS char* tb = base + CV_K + ((i16 & 1) ? 0 : CV_U - 2) + 2 * (2031 + 8 * g - i16) - 512 * 7;
  LAS char* rb = base + 480 + 32 * i16 + 16 * g;
  f32x4 acc[8];
#pragma unroll
  for (int j = 0; j < 8; ++j) acc[j] = (f32x4){0.f, 0.f, 0.f, 0.f};
#pragma unroll 2
  for (int r = 0; r < 8; ++r) {
    bf16x8 T[8], R[8];
#pragma unroll
    for (int a = 0; a < 8; ++a) {
      LAS unsigned* tp = (LAS unsigned*)(tb + 512 * (7 - a) - 64 * r);
      T[a] = __builtin_bit_cast(bf16x8, (u32x4){tp[0], tp[1], tp[2], tp[3]});
      R[a] = *(LAS bf16x8*)(rb + 512 * a - 64 * r);
    }
#pragma unroll
    for (int a = 0; a < 8; ++a)
#pragma unroll
      for (int e = 0; e + a < 8; ++e) acc[a + e] = mfma16(T[a], R[e], acc[a + e]);
  }
  STAMP(3);
#pragma unroll
  for (int jb = 0; jb < 8; ++jb) {
    const int t = 256 * jb + 16 * i16 + 4 * g;
    const u32x2 uw = *(LAS u32x2*)(base + CV_K + 2 * (2044 - t));
    const float y0 = acc[jb][0] + bf_hi(uw.y) * dcoef, y1 = acc[jb][1] + bf_lo(uw.y) * dcoef;
    const float y2 = acc[jb][2] + bf_hi(uw.x) * dcoef, y3 = acc[jb][3] + bf_lo(uw.x) * dcoef;
    *(u32x2*)(yT + (size_t)d * S + t) = (u32x2){pack_bf16(y0, y1), pack_bf16(y2, y3)};
  }
  float cs[8];
#pragma unroll
  for (int c = 0; c < 8; ++c) {
    float s = 0.f;
#pragma unroll
    for (int i = 0; i < 8; ++i) s += (c & 1) ? bf_hi(wv[i][c >> 1]) : bf_lo(wv[i][c >> 1]);
    s += __shfl_xor(s, 4);
    s += __shfl_xor(s, 8);
    cs[c] = xsum_g(s);
  }
  LAS float* red = (LAS float*)(lds + 8 * CV_W);
  if (lane < 4) {
    *(LAS f32x4*)(red + wave * 32 + lane * 8) = (f32x4){cs[0], cs[1], cs[2], cs[3]};
    *(LAS f32x4*)(red + wave * 32 + lane * 8 + 4) = (f32x4){cs[4], cs[5], cs[6], cs[7]};
  }
  WAIT_LGKM0();
  BAR();
  asm volatile("" ::: "memory");
  if (wave == 0 && lane < 32 && cb < F / 32) {
    float s = 0.f;
#pragma unroll
    for (int w = 0; w < 8; ++w) s += red[w * 32 + lane];
    colsum3[cb * 32 + lane] = s;
  }
}

__global__ __launch_bounds__(512, 2) void k_g2(const bf16_t* __restrict__ hs, const bf16_t* __restrict__ w2, const bf16_t* __restrict__ x,
                                               const f32x2* __restrict__ stats1, bf16_t* __restrict__ x1, f32x2* __restrict__ stats2,
                                               const bf16_t* __restrict__ uT, const bf16_t* __restrict__ ssk, const bf16_t* __restrict__ ssd,
                                               const bf16_t* __restrict__ w3, bf16_t* __restrict__ yT, float* __restrict__ colsum3) {
  extern __shared__ __attribute__((aligned(16))) char smem[];
  LAS char* lds = (LAS char*)smem;
  PROF_DECL;
  const int wave = __builtin_amdgcn_readfirstlane(threadIdx.x >> 6), lane = threadIdx.x & 63;
  const int xcd = blockIdx.x & 7, idx = blockIdx.x >> 3;
  if (idx & 1) {
    conv_block(lds, xcd * 16 + (idx >> 1), wave, lane, uT, ssk, ssd, w3, yT, colsum3 PROF_PASS);
    PROF_END(2);
    return;
  }
  const int pm = (xcd >> 1) * 4 + ((idx >> 1) & 3), pn = (xcd & 1) * 4 + (idx >> 3);
  constexpr int nk = D / 64;
  if (wave >= 4) {
    Tail2 tl{(const char*)(x + (size_t)pm * 128 * D + pn * 128)};
    gemm_loader<C2>(lds, (const char*)(hs + (size_t)pm * 128 * D), D * 2, (const char*)(w2 + pn * 128), D * 2, nk, wave - 4, lane, tl PROF_PASS);
    PROF_END(2);
    return;
  }
  const int wr = wave >> 1, wc = wave & 1, g = lane >> 4, mm = lane & 15;
  f32x2 st[4];
#pragma unroll
  for (int m = 0; m < 4; ++m) st[m] = stats1[pm * 128 + wr * 64 + m * 16 + mm];
  f32x4 acc[4][4], dum[4], dum1;
#pragma unroll
  for (int m = 0; m < 4; ++m)
#pragma unroll
    for (int f = 0; f < 4; ++f) acc[m][f] = (f32x4){0.f, 0.f, 0.f, 0.f};
  auto nfmap = [&](int f) { return f; };
  gemm_consumer<C2, false>(lds, nk, wr, wc, lane, acc, nfmap, dum, dum, dum1 PROF_PASS);
  LAS char* xt = lds + tail_buf<C2>(nk, 0) * C2::STAGE;
#pragma unroll
  for (int m = 0; m < 4; ++m) {
    const int rl = wr * 64 + m * 16 + mm, row = pm * 128 + rl;
    const float r = st[m].y, nmr = -st[m].x * r;
    u32x2 pk[4];
    float v[16], sum = 0.f;
#pragma unroll
    for (int f = 0; f < 4; ++f) {
      const u32x2 xw = read_row_tile<128>(xt, rl, wc * 64 + f * 16 + 4 * g);
      pk[f] = (u32x2){pack_bf16(acc[m][f][0] + (bf_lo(xw.x) * r + nmr), acc[m][f][1] + (bf_hi(xw.x) * r + nmr)),
                      pack_bf16(acc[m][f][2] + (bf_lo(xw.y) * r + nmr), acc[m][f][3] + (bf_hi(xw.y) * r + nmr))};
      v[4 * f] = bf_lo(pk[f].x), v[4 * f + 1] = bf_hi(pk[f].x), v[4 * f + 2] = bf_lo(pk[f].y), v[4 * f + 3] = bf_hi(pk[f].y);
      sum += (v[4 * f] + v[4 * f + 1]) + (v[4 * f + 2] + v[4 * f + 3]);
    }
#pragma unroll
    for (int f = 0; f < 4; f += 2) {
      const u32x4 o = swap_pair(pk[f], pk[f + 1]);
      *(u32x4*)(x1 + (size_t)row * D + pn * 128 + wc * 64 + ((f + (g & 1)) << 4) + 8 * (g >> 1)) = o;
    }
    const float mw = xsum_g(sum) * (1.0f / 64.0f);
    float m2 = 0.f;
#pragma unroll
    for (int j = 0; j < 16; ++j) m2 += (v[j] - mw) * (v[j] - mw);
    m2 = xsum_g(m2);
    if (g == 0) stats2[row * 16 + pn * 2 + wc] = (f32x2){mw, m2};
  }
  PROF_END(2);
}

typedef Cfg<224, 2, 2, 3> C3;
constexpr int LDS3 = C3::NS * C3::STAGE;
__global__ __launch_bounds__(512, 2) void k_g3(const bf16_t* __restrict__ x1, const bf16_t* __restrict__ w3, const f32x2* __restrict__ stats2,
                                               const float* __restrict__ colsum3, bf16_t* __restrict__ h3) {
  extern __shared__ __attribute__((aligned(16))) char smem[];
  LAS char* lds = (LAS char*)smem;
  PROF_DECL;
  const int wave = __builtin_amdgcn_readfirstlane(threadIdx.x >> 6), lane = threadIdx.x & 63;
  const int xcd = blockIdx.x & 7, idx = blockIdx.x >> 3;
  const int pm = (xcd >> 2) * 8 + (idx & 7), pn = (xcd & 3) * 4 + (idx >> 3);
  constexpr int nk = D / 64;
  if (wave >= 4) {
    gemm_loader<C3>(lds, (const char*)(x1 + (size_t)pm * 128 * D), D * 2, (const char*)(w3 + pn * 224), F * 2, nk, wave - 4, lane, NoTail() PROF_PASS);
    PROF_END(3);
    return;
  }
  const int wr = wave >> 1, wc = wave & 1, g = lane >> 4, mm = lane & 15;
  auto nfmap = [&](int f) { return f + wc == 7 ? 0 : f + wc; };
  float rs[4];
  f32x4 acc[4][7], dum[4], dum1;
  {
    f32x4 cw[7];
#pragma unroll
    for (int f = 0; f < 7; ++f) cw[f] = *(const f32x4*)(colsum3 + pn * 224 + wc * 112 + nfmap(f) * 16 + 4 * g);
#pragma unroll
    for (int m = 0; m < 4; ++m) {
      const f32x4* sp = (const f32x4*)(stats2 + (pm * 128 + wr * 64 + m * 16 + mm) * 16 + 4 * g);
      const f32x4 p0 = sp[0], p1 = sp[1];
      const float mu = xsum_g((p0[0] + p0[2]) + (p1[0] + p1[2])) * (1.0f / 16.0f);
      const float d0 = p0[0] - mu, d1 = p0[2] - mu, d2 = p1[0] - mu, d3 = p1[2] - mu;
      const float m2 = xsum_g((p0[1] + p0[3]) + (p1[1] + p1[3]) + 64.0f * ((d0 * d0 + d1 * d1) + (d2 * d2 + d3 * d3)));
      rs[m] = __builtin_amdgcn_rsqf(m2 * (1.0f / D) + EPS);
#pragma unroll
      for (int f = 0; f < 7; ++f) acc[m][f] = cw[f] * (-mu);
    }
  }
  gemm_consumer<C3, false>(lds, nk, wr, wc, lane, acc, nfmap, dum, dum, dum1 PROF_PASS);
  u32x2 last[4];
#pragma unroll
  for (int m = 0; m < 4; ++m) {
    const int row = pm * 128 + wr * 64 + m * 16 + mm;
    const float k0r = GK0 * rs[m], k1r = k0r * 0.044715f * rs[m] * rs[m], ir = 1.0f / rs[m];
    u32x2 pk[7];
#pragma unroll
    for (int f = 0; f < 7; ++f)
      pk[f] = (u32x2){pack_bf16(gelu_scaled(acc[m][f][0], k0r, k1r, ir), gelu_scaled(acc[m][f][1], k0r, k1r, ir)),
                      pack_bf16(gelu_scaled(acc[m][f][2], k0r, k1r, ir), gelu_scaled(acc[m][f][3], k0r, k1r, ir))};
#pragma unroll
    for (int f = 0; f < 6; f += 2) {
      const u32x4 o = swap_pair(pk[f], pk[f + 1]);
      *(u32x4*)(h3 + (size_t)row * F + pn * 224 + wc * 128 + ((f + (g & 1)) << 4) + 8 * (g >> 1)) = o;
    }
    last[m] = pk[6];
    if (m & 1) {
      const u32x4 o = swap_pair(last[m - 1], last[m]);
      *(u32x4*)(h3 + (size_t)(row - 16 + 16 * (g & 1)) * F + pn * 224 + 96 + wc * 16 + 8 * (g >> 1)) = o;
    }
  }
  PROF_END(3);
}

typedef Cfg<64, 4, 1, 6> C4;
constexpr int LDS4 = C4::NS * C4::STAGE;
struct Tail4 {
  const char* xs;
  const char* x1s;
  const char* ys;
  static constexpr int sum(int j) { return 4 * (j < 3 ? j : 3); }
  template <int J> DI void issue(LAS char* dst, int lw, int lane) const {
    if constexpr (J == 0) load_row_tile<64>(dst, xs, D * 2, lw, lane);
    if constexpr (J == 1) load_row_tile<64>(dst, x1s, D * 2, lw, lane);
    if constexpr (J == 2) {
#pragma unroll
      for (int i = 0; i < 4; ++i) {
        const int row = (lw + 4 * i) * 4 + (lane >> 4), pos = lane & 15;
        glds16(ys + (size_t)row * (S * 2) + ((pos ^ ((row & 7) << 1)) << 4), dst + (lw + 4 * i) * 1024);
      }
    }
  }
};
__global__ __launch_bounds__(512, 2) void k_g4(const bf16_t* __restrict__ h3, const bf16_t* __restrict__ w4, const bf16_t* __restrict__ x,
                                               const bf16_t* __restrict__ x1, const bf16_t* __restrict__ yT, const f32x2* __restrict__ stats1,
                                               bf16_t* __restrict__ out) {
  extern __shared__ __attribute__((aligned(16))) char smem[];
  LAS char* lds = (LAS char*)smem;
  PROF_DECL;
  const int wave = __builtin_amdgcn_readfirstlane(threadIdx.x >> 6), lane = threadIdx.x & 63;
  const int xcd = blockIdx.x & 7, idx = blockIdx.x >> 3;
  const int pm = (xcd >> 1) * 4 + (idx & 3), pn = (xcd & 1) * 8 + (idx >> 2);
  constexpr int nk = F / 64;
  if (wave >= 4) {
    Tail4 tl{(const char*)(x + (size_t)pm * 128 * D + pn * 64), (const char*)(x1 + (size_t)pm * 128 * D + pn * 64),
             (const char*)(yT + (size_t)pn * 64 * S + pm * 128)};
    gemm_loader<C4>(lds, (const char*)(h3 + (size_t)pm * 128 * F), F * 2, (const char*)(w4 + pn * 64), D * 2, nk, wave - 4, lane, tl PROF_PASS);
    PROF_END(4);
    return;
  }
  const int wr = wave, g = lane >> 4, mm = lane & 15;
  f32x2 st[2];
#pragma unroll
  for (int m = 0; m < 2; ++m) st[m] = stats1[pm * 128 + wr * 32 + m * 16 + mm];
  f32x4 acc[2][4], dum[2], dum1;
#pragma unroll
  for (int m = 0; m < 2; ++m)
#pragma unroll
    for (int f = 0; f < 4; ++f) acc[m][f] = (f32x4){0.f, 0.f, 0.f, 0.f};
  auto nfmap = [&](int f) { return f; };
  gemm_consumer<C4, false>(lds, nk, wr, 0, lane, acc, nfmap, dum, dum, dum1 PROF_PASS);
  LAS char* xt = lds + tail_buf<C4>(nk, 0) * C4::STAGE;
  LAS char* x1t = lds + tail_buf<C4>(nk, 1) * C4::STAGE;
  LAS char* yt = lds + tail_buf<C4>(nk, 2) * C4::STAGE;
#pragma unroll
  for (int m = 0; m < 2; ++m) {
    const int rl = wr * 32 + m * 16 + mm, row = pm * 128 + rl;
    const float r = st[m].y, nmr = -st[m].x * r;
    u32x2 pk[4];
#pragma unroll
    for (int f = 0; f < 4; ++f) {
      const u32x2 xw = read_row_tile<64>(xt, rl, f * 16 + 4 * g);
      const u32x2 x1w = read_row_tile<64>(x1t, rl, f * 16 + 4 * g);
      const int yrow = f * 16 + 4 * g + (mm >> 2);
      const u32x2 yw = __builtin_bit_cast(u32x2, tr_read(yt + yrow * 256 + (((wr * 2 + m) ^ (yrow & 7)) << 5) + 8 * (mm & 3)));
      const float xv[4] = {bf_lo(xw.x), bf_hi(xw.x), bf_lo(xw.y), bf_hi(xw.y)};
      const float x1v[4] = {bf_lo(x1w.x), bf_hi(x1w.x), bf_lo(x1w.y), bf_hi(x1w.y)};
      const float yv[4] = {bf_lo(yw.x), bf_hi(yw.x), bf_lo(yw.y), bf_hi(yw.y)};
      float o[4];
#pragma unroll
      for (int j = 0; j < 4; ++j) {
        const float z = x1v[j] + acc[m][f][j];
        const float gt = z * __builtin_amdgcn_rcpf(1.0f + __builtin_amdgcn_exp2f(z * -1.4426950408889634f));
        o[j] = xv[j] + (xv[j] * r + nmr) * yv[j] * gt;
      }
      pk[f] = (u32x2){pack_bf16(o[0], o[1]), pack_bf16(o[2], o[3])};
    }
#pragma unroll
    for (int f = 0; f < 4; f += 2) {
      const u32x4 o = swap_pair(pk[f], pk[f + 1]);
      *(u32x4*)(out + (size_t)row * D + pn * 64 + ((f + (g & 1)) << 4) + 8 * (g >> 1)) = o;
    }
  }
  PROF_END(4);
}

struct PArgs { const char* p[8]; };
template <int NT> __global__ __launch_bounds__(512) void k_probe(const char* p0, const char* p1, const char* p2, const char* p3, const char* p4, const char* p5, const char* p6, const char* p7, unsigned* sink) {
  const int t = blockIdx.x * 512 + threadIdx.x;
  unsigned r = 0;
  const char* q = (t >> 15) == 0 ? p0 : (t >> 15) == 1 ? p1 : (t >> 15) == 2 ? p2 : p3;
  q += (size_t)(t & 32767) * 128;
  if (NT) asm volatile("global_load_dword %0, %1, off nt" : "+v"(r) : "v"(q) : "memory");
  else asm volatile("global_load_dword %0, %1, off" : "+v"(r) : "v"(q) : "memory");
  const char* q2 = t < 57344 ? p6 + (size_t)t * 128 : t < 114688 ? p7 + (size_t)(t - 57344) * 128 : p4 + (size_t)(t - 114688) * 128;
  if (NT) asm volatile("global_load_dword %0, %1, off nt" : "+v"(r) : "v"(q2) : "memory");
  else asm volatile("global_load_dword %0, %1, off" : "+v"(r) : "v"(q2) : "memory");
  if ((t & 7) == 0) {
    const char* q3 = p5 + (size_t)(t >> 3) * 128;
    if (NT) asm volatile("global_load_dword %0, %1, off nt" : "+v"(r) : "v"(q3) : "memory");
    else asm volatile("global_load_dword %0, %1, off" : "+v"(r) : "v"(q3) : "memory");
  }
  asm volatile("s_waitcnt vmcnt(0)" : "+v"(r) :: "memory");
  if (r == 0x12345678u) sink[0] = r;
}
extern "C" void kernel_launch(void* const* d_in, const int* in_sizes, int n_in, void* d_out, int out_size, void* d_ws, size_t ws_size,
                              hipStream_t stream) {
  const bf16_t* x = (const bf16_t*)d_in[0];
  const bf16_t* kk = (const bf16_t*)d_in[1];
  const bf16_t* vv = (const bf16_t*)d_in[2];
  const bf16_t* shk = (const bf16_t*)d_in[3];
  const bf16_t* ssk = (const bf16_t*)d_in[4];
  const bf16_t* ssd = (const bf16_t*)d_in[5];
  const bf16_t* w1 = (const bf16_t*)d_in[7];
  const bf16_t* w2 = (const bf16_t*)d_in[8];
  const bf16_t* w3 = (const bf16_t*)d_in[9];
  const bf16_t* w4 = (const bf16_t*)d_in[10];
  char* ws = (char*)d_ws;
  constexpr size_t SD = (size_t)S * D * 2;
  bf16_t* uT = (bf16_t*)(ws);
  bf16_t* hs = (bf16_t*)(ws + SD);
  bf16_t* yT = (bf16_t*)(ws + 2 * SD);
  bf16_t* x1 = (bf16_t*)(ws + 3 * SD);
  bf16_t* h3 = (bf16_t*)(ws + 4 * SD);
  char* tail = ws + 4 * SD + (size_t)S * F * 2;
  f32x2* stats1 = (f32x2*)tail;
  f32x2* stats2 = (f32x2*)(tail + S * 8);
  float* colsum3 = (float*)(tail + S * 8 + S * 128);
  (void)hipFuncSetAttribute((const void*)k_g1, hipFuncAttributeMaxDynamicSharedMemorySize, LDS1);
  (void)hipFuncSetAttribute((const void*)k_g2, hipFuncAttributeMaxDynamicSharedMemorySize, LDS2);
  (void)hipFuncSetAttribute((const void*)k_g3, hipFuncAttributeMaxDynamicSharedMemorySize, LDS3);
  (void)hipFuncSetAttribute((const void*)k_g4, hipFuncAttributeMaxDynamicSharedMemorySize, LDS4);
  k_probe<0><<<256, 512, 0, stream>>>((const char*)x, (const char*)kk, (const char*)vv, (const char*)ssk, (const char*)w1, (const char*)w2, (const char*)w3, (const char*)w4, (unsigned*)(ws + (200u << 20)));
  k_prep<<<128, 256, 0, stream>>>(kk, vv, shk, uT);
  k_g1<<<256, 512, LDS1, stream>>>(x, w1, hs, stats1);
  k_g2<<<256, 512, LDS2, stream>>>(hs, w2, x, stats1, x1, stats2, uT, ssk, ssd, w3, yT, colsum3);
  k_g3<<<256, 512, LDS3, stream>>>(x1, w3, stats2, colsum3, h3);
  k_g4<<<256, 512, LDS4, stream>>>(h3, w4, x, x1, yT, stats1, (bf16_t*)d_out);
}
```

```cpp
#include <hip/hip_runtime.h>
#include <stdint.h>

#define LAS __attribute__((address_space(3)))
#define DI __device__ __forceinline__
typedef unsigned short bf16_t;
typedef __bf16 bf16x8 __attribute__((ext_vector_type(8)));
typedef __bf16 bf16x2 __attribute__((ext_vector_type(2)));
typedef short s16x4 __attribute__((ext_vector_type(4)));
typedef float f32x2 __attribute__((ext_vector_type(2)));
typedef float f32x4 __attribute__((ext_vector_type(4)));
typedef unsigned u32x2 __attribute__((ext_vector_type(2)));
typedef unsigned u32x4 __attribute__((ext_vector_type(4)));
typedef unsigned u32x4u __attribute__((ext_vector_type(4), aligned(2)));
typedef __amdgpu_buffer_rsrc_t rsrc_t;

constexpr int S = 2048, D = 1024, F = 3584;
constexpr float EPS = 1e-5f;
constexpr int AUX_NT = 2, AUX_SC1 = 16;
#define AUX_HS 0
#define AUX_X1 0
#define AUX_H3 0
#define AUX_OUT 0

#define PROF_ARG
#define PROF_PASS
#define PROF_DECL
#define STAMP(i)
#define PROF_END(kid)
DI float bf_lo(unsigned w) { return __uint_as_float(w << 16); }
DI float bf_hi(unsigned w) { return __uint_as_float(w & 0xffff0000u); }
DI f32x2 bf_pair(unsigned w) { return (f32x2){bf_lo(w), bf_hi(w)}; }
DI unsigned pack2(f32x2 v) { return __builtin_bit_cast(unsigned, __builtin_convertvector(v, bf16x2)); }
DI unsigned pack_bf16(float lo, float hi) { return pack2((f32x2){lo, hi}); }
DI f32x2 lo2(f32x4 v) { return __builtin_shufflevector(v, v, 0, 1); }
DI f32x2 hi2(f32x4 v) { return __builtin_shufflevector(v, v, 2, 3); }
DI f32x2 splat(float v) { return (f32x2){v, v}; }
DI f32x2 fma2(f32x2 a, f32x2 b, f32x2 c) { return __builtin_elementwise_fma(a, b, c); }
#define BAR() __builtin_amdgcn_s_barrier()
#define WAIT_LGKM0() asm volatile("s_waitcnt lgkmcnt(0)" ::: "memory")
template <int N> DI void wait_vm() { asm volatile("s_waitcnt vmcnt(%0)" ::"n"(N) : "memory"); }
template <int AUX = 0> DI void glds16(const void* src, LAS char* dst) {
  __builtin_amdgcn_global_load_lds((const unsigned*)src, (LAS unsigned*)dst, 16, 0, AUX);
}
DI rsrc_t mk_rsrc(const void* p, unsigned bytes) { return __builtin_amdgcn_make_buffer_rsrc((void*)p, (short)0, (int)bytes, 0x00020000); }
template <int AUX> DI void st16(rsrc_t r, int off, u32x4 v) { __builtin_amdgcn_raw_buffer_store_b128(v, r, off, 0, AUX); }
template <int AUX> DI u32x4 ld16(rsrc_t r, int off) { return __builtin_amdgcn_raw_buffer_load_b128(r, off, 0, AUX); }
DI s16x4 tr_read(LAS char* p) { return __builtin_amdgcn_ds_read_tr16_b64_v4i16((LAS s16x4*)p); }
DI bf16x8 tr_frag(LAS char* p, int step) {
  const s16x4 lo = tr_read(p), hi = tr_read(p + step);
  return __builtin_bit_cast(bf16x8, __builtin_shufflevector(lo, hi, 0, 1, 2, 3, 4, 5, 6, 7));
}
DI f32x4 mfma16(bf16x8 a, bf16x8 b, f32x4 c) { return __builtin_amdgcn_mfma_f32_16x16x32_bf16(a, b, c, 0, 0, 0); }
DI float red16(float a, float b) {
  const auto r = __builtin_amdgcn_permlane16_swap(__float_as_uint(a), __float_as_uint(b), false, false);
  return __uint_as_float(r[0]) + __uint_as_float(r[1]);
}
DI float red32(float a, float b) {
  const auto r = __builtin_amdgcn_permlane32_swap(__float_as_uint(a), __float_as_uint(b), false, false);
  return __uint_as_float(r[0]) + __uint_as_float(r[1]);
}
DI f32x2 gelu2(f32x2 a, float k0r, float k1r, float ir) {
  const f32x2 z = a * fma2(a * a, splat(k1r), splat(k0r));
  const f32x2 e = {__builtin_amdgcn_exp2f(z.x), __builtin_amdgcn_exp2f(z.y)};
  const f32x2 d = fma2(e, splat(ir), splat(ir));
  return a * (f32x2){__builtin_amdgcn_rcpf(d.x), __builtin_amdgcn_rcpf(d.y)};
}
constexpr float GK0 = -2.0f * 0.7978845608028654f * 1.4426950408889634f;
DI u32x4 swap_pair(u32x2 x, u32x2 y) {
  const auto a = __builtin_amdgcn_permlane16_swap(x.x, y.x, false, false);
  const auto b = __builtin_amdgcn_permlane16_swap(x.y, y.y, false, false);
  return (u32x4){a[0], b[0], a[1], b[1]};
}

template <int BN_, int WR_, int WC_, int NS_, int AM_> struct Cfg {
  static constexpr int BN = BN_, WR = WR_, WC = WC_, NS = NS_, AM = AM_;
  static constexpr int TM = 128 / WR, TN = BN / WC, MF = TM / 16, NF = TN / 16;
  static constexpr int ABYTES = 128 * 64 * 2, BBYTES = 64 * BN * 2, STAGE = ABYTES + BBYTES;
  static constexpr int NB = BN / 32, PER = 4 + NB, RB = BN * 2, CPR = BN / 8;
  static DI int swzB(int k) {
    return BN == 64 ? (((k >> 1) & 1) | (((k >> 3) & 1) << 1)) : BN == 128 ? ((2 * (k & 3)) | ((k >> 3) & 1)) : ((k >> 3) & 1);
  }
};

template <class C, class Tail, int J> DI void loader_tail_iters(LAS char* lds, int& buf, int lw, int lane, const Tail& tail) {
  if constexpr (J < C::NS - 1) {
    wait_vm<(C::NS - 2 - J) * C::PER + Tail::sum(J)>();
    BAR();
    tail.template issue<J>(lds + buf * C::STAGE, lw, lane);
    buf = (buf + 1 == C::NS) ? 0 : buf + 1;
    loader_tail_iters<C, Tail, J + 1>(lds, buf, lw, lane, tail);
  }
}

template <class C, class Tail, class Mid>
DI void gemm_loader(LAS char* lds, const char* Ab, int lda_b, const char* Bb, int ldw_b, int nk, int phi, int lw, int lane, const Tail& tail,
                    const Mid& mid PROF_ARG) {
  const unsigned voffA = C::AM == 0 ? (unsigned)((8 * lw + (lane >> 3)) * lda_b + (((lane & 7) ^ ((4 * lw + (lane >> 4)) & 7)) << 4))
                                    : (unsigned)(lw * 1024 + lane * 16);
  unsigned voffB[C::NB];
#pragma unroll
  for (int i = 0; i < C::NB; ++i) {
    const int L = (lw + 4 * i) * 64 + lane, k = L / C::CPR, pos = L % C::CPR;
    voffB[i] = (unsigned)(k * ldw_b + ((pos ^ (C::swzB(k) << 1)) << 4));
  }
  unsigned done = 0;
  auto issue = [&](int t, int buf) {
    const int kt = ((t & 3) == phi && t + 3 < nk) ? t + 3 : __builtin_ctz(~done);
    done |= 1u << kt;
    const char* a = Ab + (C::AM == 0 ? kt * 128 : kt * 16384);
    const char* b = Bb + (size_t)kt * 64 * ldw_b;
    LAS char* dst = lds + buf * C::STAGE + lw * 1024;
#pragma unroll
    for (int i = 0; i < 4; ++i) glds16(a + (C::AM == 0 ? (size_t)i * 32 * lda_b : (size_t)i * 4096) + voffA, dst + i * 4096);
#pragma unroll
    for (int i = 0; i < C::NB; ++i) glds16(b + voffB[i], dst + C::ABYTES + i * 4096);
  };
#pragma unroll
  for (int s = 0; s < C::NS - 1; ++s) issue(s, s);
  mid();
  int buf = C::NS - 1;
  STAMP(1);
  for (int t = C::NS - 1; t < nk; ++t) {
    wait_vm<(C::NS - 2) * C::PER>();
    BAR();
    issue(t, buf);
    buf = (buf + 1 == C::NS) ? 0 : buf + 1;
  }
  STAMP(2);
  loader_tail_iters<C, Tail, 0>(lds, buf, lw, lane, tail);
  wait_vm<Tail::LEFT>();
  BAR();
}
template <class C> constexpr int tail_buf(int nk, int j) { return (nk + j) % C::NS; }

template <int NM, int NA, int NBF> DI void sched_mix() {
#pragma unroll
  for (int i = 0; i < NA; ++i) {
    __builtin_amdgcn_sched_group_barrier(0x8, 1, 0);
    __builtin_amdgcn_sched_group_barrier(0x100, 1, 0);
  }
#pragma unroll
  for (int i = 0; i < NBF; ++i) {
    __builtin_amdgcn_sched_group_barrier(0x8, 1, 0);
    __builtin_amdgcn_sched_group_barrier(0x100, 2, 0);
  }
  __builtin_amdgcn_sched_group_barrier(0x8, NM - NA - NBF, 0);
}
template <int V> struct IC { static constexpr int v = V; };

template <class C, int NK, bool STATS, class NfMap, class First>
DI void gemm_consumer(LAS char* lds, int wr, int wc, int lane, f32x4 (&acc)[C::MF][C::NF], const NfMap& nfmap, f32x4 (&rsum)[C::MF],
                      f32x4 (&rsq)[C::MF], f32x4& csum, const First& first PROF_ARG) {
  const int g = lane >> 4, q = (lane & 15) >> 2, p = lane & 3;
  const int aoff0 = C::AM == 0 ? (wr * C::TM + (lane & 15)) * 128 + ((g ^ ((lane >> 1) & 7)) << 4)
                               : g * 2048 + (wr * (C::AM == 1 ? C::TM : 32) + (lane & 15)) * 16;
  const int aoff1 = C::AM == 0 ? aoff0 ^ 64 : aoff0 + 8192;
  int boff[C::NF];
#pragma unroll
  for (int f = 0; f < C::NF; ++f) {
    const int k = 8 * g + q;
    boff[f] = C::ABYTES + k * C::RB + (((wc * C::NF + nfmap(f)) ^ C::swzB(k)) << 5) + 8 * p;
  }
  bf16x8 a0[C::MF], b0[C::NF], a1[C::MF], b1[C::NF];
  const bf16x8 ones = __builtin_bit_cast(bf16x8, (u32x4){0x3f803f80u, 0x3f803f80u, 0x3f803f80u, 0x3f803f80u});
  auto ldfr = [&](bf16x8(&a)[C::MF], bf16x8(&b)[C::NF], LAS char* st, int ks) {
#pragma unroll
    for (int m = 0; m < C::MF; ++m)
      a[m] = *(LAS bf16x8*)(st + (ks ? aoff1 : aoff0) + (C::AM == 0 ? m * 2048 : C::AM == 1 ? m * 256 : (m >> 1) * 1024 + (m & 1) * 256));
#pragma unroll
    for (int f = 0; f < C::NF; ++f) b[f] = tr_frag(st + boff[f] + ks * 32 * C::RB, 4 * C::RB);
  };
  auto mma = [&](bf16x8(&a)[C::MF], bf16x8(&b)[C::NF]) {
    if constexpr (STATS) {
#pragma unroll
      for (int m = 0; m < C::MF; ++m) {
        rsum[m] = mfma16(ones, a[m], rsum[m]);
        rsq[m] = mfma16(a[m], a[m], rsq[m]);
      }
      csum = mfma16(b[0], ones, csum);
    }
#pragma unroll
    for (int f = 0; f < C::NF; ++f)
#pragma unroll
      for (int m = 0; m < C::MF; ++m) acc[m][f] = mfma16(b[f], a[m], acc[m][f]);
  };
  constexpr int NM = C::MF * C::NF + (STATS ? 2 * C::MF + 1 : 0);
  auto step = [&](auto slot) {
    constexpr int SL = decltype(slot)::v;
    ldfr(a1, b1, lds + SL * C::STAGE, 1);
    mma(a0, b0);
    sched_mix<NM, C::MF, C::NF>();
    __builtin_amdgcn_sched_barrier(0);
    WAIT_LGKM0();
    BAR();
    asm volatile("" ::: "memory");
    __builtin_amdgcn_sched_barrier(0);
    ldfr(a0, b0, lds + ((SL + 1) % C::NS) * C::STAGE, 0);
    mma(a1, b1);
    sched_mix<NM, C::MF, C::NF>();
    __builtin_amdgcn_sched_barrier(0);
  };
  STAMP(1);
  WAIT_LGKM0();
  BAR();
  asm volatile("" ::: "memory");
  STAMP(2);
  ldfr(a0, b0, lds, 0);
  first();
  __builtin_amdgcn_sched_barrier(0);
#pragma unroll 1
  for (int it = 0; it < NK / C::NS; ++it) {
    step(IC<0>());
    step(IC<1>());
    step(IC<2>());
    if constexpr (C::NS > 3) step(IC<3 % C::NS>());
    if constexpr (C::NS > 4) step(IC<4 % C::NS>());
  }
  static_assert(NK % C::NS <= 1 && C::NS <= 5, "ring");
  if constexpr (NK % C::NS == 1) step(IC<0>());
  STAMP(3);
}

struct NoOp { DI void operator()() const {} };
struct NoTail {
  static constexpr int LEFT = 0;
  static constexpr int sum(int) { return 0; }
  template <int J> DI void issue(LAS char*, int, int) const {}
};
template <int ROWS> DI void load_row_tile(LAS char* dst, const char* src, int ld_b, int lw, int lane) {
#pragma unroll
  for (int i = 0; i < ROWS / 16; ++i) {
    const int row = (lw + 4 * i) * 4 + (lane >> 4), pos = lane & 15;
    glds16(src + (size_t)row * ld_b + ((pos ^ (row & 15)) << 4), dst + (lw + 4 * i) * 1024);
  }
}
DI u32x2 read_row_tile(LAS char* t, int row, int col) {
  return *(LAS u32x2*)(t + row * 256 + (((col >> 3) ^ (row & 15)) << 4) + ((col & 4) << 1));
}

typedef Cfg<64, 4, 1, 5, 0> C1;
constexpr int L1_U = C1::NS * C1::STAGE, L1_CS = L1_U + 16384, LDS1 = L1_CS + 256;
struct Tail1 {
  const char* w;
  static constexpr int LEFT = 8;
  static constexpr int sum(int j) { return j > 0 ? 8 : 0; }
  template <int J> DI void issue(LAS char* dst, int lw, int lane) const {
    if constexpr (J == 0) {
#pragma unroll
      for (int i = 0; i < 8; ++i) glds16(w + (lw * 8 + i) * (D * 2) + lane * 16, dst + lw * 1024);
    }
  }
};
__global__ __launch_bounds__(512, 2) void k_g1(const bf16_t* __restrict__ x, const bf16_t* __restrict__ w1, const bf16_t* __restrict__ w2,
                                               const bf16_t* __restrict__ kk, const bf16_t* __restrict__ vv, const bf16_t* __restrict__ shk,
                                               bf16_t* __restrict__ hs, bf16_t* __restrict__ uT, f32x2* __restrict__ stats1) {
  extern __shared__ __attribute__((aligned(16))) char smem[];
  LAS char* lds = (LAS char*)smem;
  PROF_DECL;
  const int wave = __builtin_amdgcn_readfirstlane(threadIdx.x >> 6), lane = threadIdx.x & 63;
  const int xcd = blockIdx.x & 7, idx = blockIdx.x >> 3;
  const int pm = (xcd >> 1) * 4 + (idx & 3), pn = (xcd & 1) * 8 + (idx >> 2);
  constexpr int nk = D / 64;
  if (wave >= 4) {
    Tail1 tl{(const char*)(w2 + (size_t)idx * 32 * D + (xcd & 1) * 512)};
    gemm_loader<C1>(lds, (const char*)(x + (size_t)pm * 128 * D), D * 2, (const char*)(w1 + pn * 64), D * 2, nk, (idx + (idx >> 2)) & 3, wave - 4,
                    lane, tl, NoOp() PROF_PASS);
    BAR();
    wait_vm<0>();
    PROF_END(1);
    return;
  }
  const int wr = wave;
  {
    const int tq = lane >> 3, dq = lane & 7, t0 = pm * 128 + wr * 32 + tq * 4, d0 = pn * 64 + dq * 8;
    const rsrc_t rk = mk_rsrc(kk, S * D * 2), rv = mk_rsrc(vv, S * D * 2);
    u32x4 kr[7], vr[4], sk[4];
#pragma unroll
    for (int j = 0; j < 4; ++j) sk[j] = *(const u32x4*)(shk + d0 * 4 + j * 8);
#pragma unroll
    for (int j = 0; j < 7; ++j) kr[j] = ld16<AUX_NT>(rk, (max(t0 - 3 + j, 0) * D + d0) * 2);
#pragma unroll
    for (int j = 0; j < 4; ++j) vr[j] = ld16<AUX_NT>(rv, ((t0 + j) * D + d0) * 2);
    if (t0 == 0) kr[0] = kr[1] = kr[2] = (u32x4){0u, 0u, 0u, 0u};
    LAS char* ub = lds + L1_U + dq * 2048 + (((15 - 4 * wr - (tq >> 1)) ^ dq) << 4) + ((~tq & 1) << 3);
#pragma unroll
    for (int c = 0; c < 4; ++c) {
      const f32x2 s0 = {bf_lo(sk[c][0]), bf_lo(sk[c][2])}, s1 = {bf_hi(sk[c][0]), bf_hi(sk[c][2])};
      const f32x2 s2 = {bf_lo(sk[c][1]), bf_lo(sk[c][3])}, s3 = {bf_hi(sk[c][1]), bf_hi(sk[c][3])};
      f32x2 kf[7], u[4];
#pragma unroll
      for (int j = 0; j < 7; ++j) kf[j] = bf_pair(kr[j][c]);
#pragma unroll
      for (int j = 0; j < 4; ++j) u[j] = fma2(kf[j + 3], s0, fma2(kf[j + 2], s1, fma2(kf[j + 1], s2, kf[j] * s3))) * bf_pair(vr[j][c]);
      *(LAS u32x2*)(ub + (2 * c) * 256) = (u32x2){pack_bf16(u[3].x, u[2].x), pack_bf16(u[1].x, u[0].x)};
      *(LAS u32x2*)(ub + (2 * c + 1) * 256) = (u32x2){pack_bf16(u[3].y, u[2].y), pack_bf16(u[1].y, u[0].y)};
    }
  }
  f32x4 acc[2][4], rsum[2], rsq[2], csum = {0.f, 0.f, 0.f, 0.f};
#pragma unroll
  for (int m = 0; m < 2; ++m) {
    rsum[m] = rsq[m] = (f32x4){0.f, 0.f, 0.f, 0.f};
#pragma unroll
    for (int f = 0; f < 4; ++f) acc[m][f] = (f32x4){0.f, 0.f, 0.f, 0.f};
  }
  auto nfmap = [&](int f) { return f ^ wr; };
  auto first = [&]() {
    const rsrc_t ru = mk_rsrc(uT, S * D * 2);
#pragma unroll
    for (int i = 0; i < 4; ++i) {
      const int d = wr * 16 + i * 4 + (lane >> 4);
      st16<AUX_SC1>(ru, ((pn * 64 + d) * S + (S - 128 - pm * 128)) * 2 + (lane & 15) * 16,
                    *(LAS u32x4*)(lds + L1_U + d * 256 + (((lane & 15) ^ (d >> 3)) << 4)));
    }
  };
  gemm_consumer<C1, nk, true>(lds, wr, 0, lane, acc, nfmap, rsum, rsq, csum, first PROF_PASS);
  const int g = lane >> 4, mm = lane & 15;
  LAS float* cs = (LAS float*)(lds + L1_CS);
  if (mm == 0) *(LAS f32x4*)(cs + wr * 16 + 4 * g) = csum;
  WAIT_LGKM0();
  BAR();
  asm volatile("" ::: "memory");
  f32x4 cw[4];
#pragma unroll
  for (int f = 0; f < 4; ++f) cw[f] = *(LAS f32x4*)(cs + (f ^ wr) * 16 + 4 * g);
  const int grp = pn >> 2, delta = (grp & 1) ? -((grp >> 1) + 1) : ((grp >> 1) + 1);
  const rsrc_t rh = mk_rsrc(hs, S * D * 2);
#pragma unroll
  for (int m = 0; m < 2; ++m) {
    const int sel = mm & 3;
    float dq = sel == 0 ? rsq[m][0] : sel == 1 ? rsq[m][1] : sel == 2 ? rsq[m][2] : rsq[m][3];
    dq = __shfl(dq, mm + 16 * (mm >> 2));
    const float mu = rsum[m][0] * (1.0f / D);
    const float var = fmaxf(dq * (1.0f / D) - mu * mu, 0.f);
    const float r = __builtin_amdgcn_rsqf(var + EPS);
    const float rr = r * __builtin_amdgcn_rsqf(var * r * r + EPS);
    const int row = pm * 128 + wr * 32 + m * 16 + mm;
    if (pn == 0 && g == 0) stats1[row] = (f32x2){mu, r};
    const float k0r = GK0 * rr, k1r = k0r * 0.044715f * rr * rr, ir = __builtin_amdgcn_rcpf(rr);
    u32x2 pk[4];
#pragma unroll
    for (int f = 0; f < 4; ++f)
      pk[f] = (u32x2){pack2(gelu2(fma2(lo2(cw[f]), splat(-mu), lo2(acc[m][f])), k0r, k1r, ir)),
                      pack2(gelu2(fma2(hi2(cw[f]), splat(-mu), hi2(acc[m][f])), k0r, k1r, ir))};
    const int drow = (row + delta) & (S - 1);
    const bool wrap = drow != row + delta;
#pragma unroll
    for (int f = 0; f < 4; f += 2) {
      u32x4 o = swap_pair(pk[f], pk[f + 1]);
      if (wrap) o = (u32x4){0u, 0u, 0u, 0u};
      const int chunk = pn * 8 + (((f + (g & 1)) ^ wr) << 1) + (g >> 1);
      st16<AUX_HS>(rh, ((drow >> 7) * (D / 8) + chunk) * 2048 + (drow & 127) * 16, o);
    }
  }
  PROF_END(1);
}

typedef Cfg<128, 2, 2, 4, 1> C2;
struct Tail2 {
  const char* xs;
  static constexpr int LEFT = 0;
  static constexpr int sum(int j) { return j > 0 ? 8 : 0; }
  template <int J> DI void issue(LAS char* dst, int lw, int lane) const {
    if constexpr (J == 0) load_row_tile<128>(dst, xs, D * 2, lw, lane);
  }
};
constexpr int CV_K = 4608, CV_U = 4160, CV_W = CV_K + 2 * CV_U;
constexpr int L2_RED = 8 * CV_W, L2_DUMMY = L2_RED + 2048;
constexpr int LDS2 = C2::NS * C2::STAGE;
static_assert(L2_DUMMY + 4096 + 16 <= LDS2, "conv LDS");
constexpr unsigned XCH_EMPTY = 0xffffffffu;

DI void conv_wave(LAS char* lds, int d, int wave, int lane, const bf16_t* __restrict__ uT, const bf16_t* __restrict__ ssk,
                  const bf16_t* __restrict__ ssd, bf16_t* __restrict__ yT PROF_ARG) {
  LAS char* base = lds + wave * CV_W;
  const char* ksrc = (const char*)(ssk + (size_t)d * S);
  const char* usrc = (const char*)(uT + (size_t)d * S);
  u32x4 sh[4];
#pragma unroll
  for (int i = 0; i < 4; ++i) glds16<AUX_NT>(ksrc + i * 1024 + lane * 16, base + 512 + i * 1024);
#pragma unroll
  for (int i = 0; i < 4; ++i) glds16(usrc + i * 1024 + lane * 16, base + CV_K + i * 1024);
#pragma unroll
  for (int i = 0; i < 4; ++i) sh[i] = *(const u32x4u*)(usrc + i * 1024 + lane * 16 + 2);
  const float dcoef = bf_lo(ssd[d]);
  BAR();
  *(LAS u32x2*)(base + lane * 8) = (u32x2){0u, 0u};
  if (lane < 32) *(LAS unsigned*)(base + CV_K + 4096 + (lane >> 4) * CV_U + (lane & 15) * 4) = 0u;
  if (lane == 63) sh[3][3] &= 0xffffu;
#pragma unroll
  for (int i = 0; i < 4; ++i) *(LAS u32x4*)(base + CV_K + CV_U + i * 1024 + lane * 16) = sh[i];
  wait_vm<0>();
  WAIT_LGKM0();
  STAMP(2);
  const int i16 = lane & 15, g = lane >> 4;
  LAS char* tb = base + CV_K + ((i16 & 1) ? 0 : CV_U - 2) + 2 * (2031 + 8 * g - i16) - 512 * 7;
  LAS char* rb = base + 480 + 32 * i16 + 16 * g;
  f32x4 acc[8];
#pragma unroll
  for (int j = 0; j < 8; ++j) acc[j] = (f32x4){0.f, 0.f, 0.f, 0.f};
#pragma unroll 2
  for (int r = 0; r < 8; ++r) {
    bf16x8 T[8], R[8];
#pragma unroll
    for (int a = 0; a < 8; ++a) {
      LAS unsigned* tp = (LAS unsigned*)(tb + 512 * (7 - a) - 64 * r);
      T[a] = __builtin_bit_cast(bf16x8, (u32x4){tp[0], tp[1], tp[2], tp[3]});
      R[a] = *(LAS bf16x8*)(rb + 512 * a - 64 * r);
    }
#pragma unroll
    for (int a = 0; a < 8; ++a)
#pragma unroll
      for (int e = 0; e + a < 8; ++e) acc[a + e] = mfma16(T[a], R[e], acc[a + e]);
  }
  STAMP(3);
#pragma unroll
  for (int jb = 0; jb < 8; ++jb) {
    const int t = 256 * jb + 16 * i16 + 4 * g;
    const u32x2 uw = *(LAS u32x2*)(base + CV_K + 2 * (2044 - t));
    const float y0 = acc[jb][0] + bf_hi(uw.y) * dcoef, y1 = acc[jb][1] + bf_lo(uw.y) * dcoef;
    const float y2 = acc[jb][2] + bf_hi(uw.x) * dcoef, y3 = acc[jb][3] + bf_lo(uw.x) * dcoef;
    *(LAS u32x2*)(base + 2 * t) = (u32x2){pack_bf16(y0, y1), pack_bf16(y2, y3)};
  }
  const rsrc_t ry = mk_rsrc(yT, S * D * 2);
#pragma unroll
  for (int i = 0; i < 4; ++i) st16<AUX_SC1>(ry, d * (S * 2) + i * 1024 + lane * 16, *(LAS u32x4*)(base + i * 1024 + lane * 16));
}

DI void aux_wave(LAS char* lds, int xcd, int ci, int aw, int lane, const bf16_t* __restrict__ w3, const bf16_t* __restrict__ w4,
                 float* __restrict__ colsum3, unsigned* __restrict__ xch) {
  const int cb = xcd * 16 + ci;
  LAS unsigned* cnt = (LAS unsigned*)(lds + L2_DUMMY + 4096);
  if (aw == 0) *cnt = 0u;
  WAIT_LGKM0();
  BAR();
  u32x4 wv[16];
  const int cc = ci < 14 ? ci : 0;
#pragma unroll
  for (int i = 0; i < 16; ++i)
    wv[i] = *(const u32x4*)(w3 + (size_t)((xcd >> 1) * 256 + aw * 64 + i * 4 + (lane >> 4)) * F + (xcd & 1) * 1792 + cc * 128 + (lane & 15) * 8);
  __builtin_amdgcn_sched_barrier(0);
#pragma unroll
  for (int i = 0; i < 14; ++i) glds16((const char*)w4 + ((cb * 4 + aw) * 14 + i) * 1024 + lane * 16, lds + L2_DUMMY + aw * 1024);
  const rsrc_t rx = mk_rsrc(xch, 256 * 16384);
#pragma unroll
  for (int i = 0; i < 8; ++i) st16<AUX_SC1>(rx, ((cb * 4 + aw) * 8 + i) * 1024 + lane * 16, (u32x4){XCH_EMPTY, XCH_EMPTY, XCH_EMPTY, XCH_EMPTY});
  __builtin_amdgcn_sched_barrier(0);
  f32x2 cs[4];
#pragma unroll
  for (int c = 0; c < 4; ++c) {
    cs[c] = bf_pair(wv[0][c]);
#pragma unroll
    for (int i = 1; i < 16; ++i) cs[c] += bf_pair(wv[i][c]);
  }
  const float sx = red32(red16(cs[0].x, cs[1].x), red16(cs[2].x, cs[3].x)), sy = red32(red16(cs[0].y, cs[1].y), red16(cs[2].y, cs[3].y));
  *(LAS f32x2*)(lds + L2_RED + aw * 512 + (lane & 15) * 32 + (lane >> 4) * 8) = (f32x2){sx, sy};
  WAIT_LGKM0();
  unsigned old = 0;
  if (lane == 0) old = __hip_atomic_fetch_add(cnt, 1u, __ATOMIC_RELAXED, __HIP_MEMORY_SCOPE_WORKGROUP);
  if (__builtin_amdgcn_readfirstlane(old) == 3 && ci < 14) {
    asm volatile("" ::: "memory");
    LAS f32x2* red = (LAS f32x2*)(lds + L2_RED);
    *(f32x2*)(colsum3 + (xcd >> 1) * F + (xcd & 1) * 1792 + ci * 128 + lane * 2) = (red[lane] + red[64 + lane]) + (red[128 + lane] + red[192 + lane]);
  }
}

__global__ __launch_bounds__(768, 3) void k_g2(const bf16_t* __restrict__ hs, const bf16_t* __restrict__ w2, const bf16_t* __restrict__ x,
                                               const f32x2* __restrict__ stats1, bf16_t* __restrict__ x1, f32x2* __restrict__ stats2,
                                               const bf16_t* __restrict__ uT, const bf16_t* __restrict__ ssk, const bf16_t* __restrict__ ssd,
                                               const bf16_t* __restrict__ w3, const bf16_t* __restrict__ w4, bf16_t* __restrict__ yT,
                                               float* __restrict__ colsum3, unsigned* __restrict__ xch) {
  extern __shared__ __attribute__((aligned(16))) char smem[];
  LAS char* lds = (LAS char*)smem;
  PROF_DECL;
  const int wave = __builtin_amdgcn_readfirstlane(threadIdx.x >> 6), lane = threadIdx.x & 63;
  const int xcd = blockIdx.x & 7, idx = blockIdx.x >> 3;
  if (idx & 1) {
    const int ci = idx >> 1;
    if (wave < 8) {
      conv_wave(lds, (xcd * 16 + ci) * 8 + wave, wave, lane, uT, ssk, ssd, yT PROF_PASS);
      PROF_END(2);
      return;
    }
    aux_wave(lds, xcd, ci, wave - 8, lane, w3, w4, colsum3, xch);
    STAMP(2);
    wait_vm<0>();
    PROF_END(2);
    return;
  }
  if (wave >= 8) return;
  const int jm = (idx >> 1) & 3, jn = idx >> 3;
  const int pm = (xcd >> 1) * 4 + jm, pn = (xcd & 1) * 4 + jn;
  constexpr int nk = D / 64;
  if (wave >= 4) {
    Tail2 tl{(const char*)(x + (size_t)pm * 128 * D + pn * 128)};
    gemm_loader<C2>(lds, (const char*)(hs + (size_t)pm * 128 * D), 0, (const char*)(w2 + pn * 128), D * 2, nk, (jm + jn) & 3, wave - 4, lane, tl,
                    NoOp() PROF_PASS);
    PROF_END(2);
    return;
  }
  const int wr = wave >> 1, wc = wave & 1, g = lane >> 4, mm = lane & 15;
  f32x2 st[4];
#pragma unroll
  for (int m = 0; m < 4; ++m) st[m] = stats1[pm * 128 + wr * 64 + m * 16 + mm];
  f32x4 acc[4][4], dum[4], dum1;
#pragma unroll
  for (int m = 0; m < 4; ++m)
#pragma unroll
    for (int f = 0; f < 4; ++f) acc[m][f] = (f32x4){0.f, 0.f, 0.f, 0.f};
  auto nfmap = [&](int f) { return f; };
  gemm_consumer<C2, nk, false>(lds, wr, wc, lane, acc, nfmap, dum, dum, dum1, NoOp() PROF_PASS);
  LAS char* xt = lds + tail_buf<C2>(nk, 0) * C2::STAGE;
  const rsrc_t r1 = mk_rsrc(x1, S * D * 2);
  float sum[4], sq[4];
#pragma unroll
  for (int m = 0; m < 4; ++m) {
    const int rl = wr * 64 + m * 16 + mm;
    const f32x2 r = splat(st[m].y), nmr = splat(-st[m].x * st[m].y);
    u32x2 pk[4];
    f32x2 s = {0.f, 0.f}, s2 = {0.f, 0.f};
#pragma unroll
    for (int f = 0; f < 4; ++f) {
      const u32x2 xw = read_row_tile(xt, rl, wc * 64 + f * 16 + 4 * g);
      const f32x2 v0 = lo2(acc[m][f]) + fma2(bf_pair(xw.x), r, nmr), v1 = hi2(acc[m][f]) + fma2(bf_pair(xw.y), r, nmr);
      pk[f] = (u32x2){pack2(v0), pack2(v1)};
      s += v0 + v1;
      s2 = fma2(v0, v0, fma2(v1, v1, s2));
    }
#pragma unroll
    for (int f = 0; f < 4; f += 2)
      st16<AUX_X1>(r1, (pm * (D / 8) + pn * 16 + wc * 8 + ((f + (g & 1)) << 1) + (g >> 1)) * 2048 + rl * 16, swap_pair(pk[f], pk[f + 1]));
    sum[m] = s.x + s.y;
    sq[m] = s2.x + s2.y;
  }
  stats2[(pn * 2 + wc) * S + pm * 128 + wr * 64 + lane] =
      (f32x2){red32(red16(sum[0], sum[1]), red16(sum[2], sum[3])), red32(red16(sq[0], sq[1]), red16(sq[2], sq[3]))};
  PROF_END(2);
}

typedef Cfg<224, 2, 2, 3, 1> C3;
constexpr int L3_ROW = C3::NS * C3::STAGE, L3_COL = L3_ROW + 2048, LDS3 = L3_COL + 1024;
__global__ __launch_bounds__(512, 2) void k_g3(const bf16_t* __restrict__ x1, const bf16_t* __restrict__ w3, const f32x2* __restrict__ stats2,
                                               const float* __restrict__ colsum3, bf16_t* __restrict__ h3) {
  extern __shared__ __attribute__((aligned(16))) char smem[];
  LAS char* lds = (LAS char*)smem;
  PROF_DECL;
  const int wave = __builtin_amdgcn_readfirstlane(threadIdx.x >> 6), lane = threadIdx.x & 63;
  const int xcd = blockIdx.x & 7, idx = blockIdx.x >> 3;
  const int pm = (xcd >> 1) * 4 + (idx & 3), pn = (xcd & 1) * 8 + (idx >> 2);
  constexpr int nk = D / 64;
  if (wave >= 4) {
    const int lw = wave - 4;
    f32x2 sp[8];
    float cp[4];
#pragma unroll
    for (int i = 0; i < 8; ++i) sp[i] = stats2[((lane >> 5) * 8 + i) * S + pm * 128 + lw * 32 + (lane & 31)];
#pragma unroll
    for (int i = 0; i < 4; ++i) cp[i] = colsum3[i * F + pn * 224 + lw * 56 + (lane < 56 ? lane : 0)];
    __builtin_amdgcn_sched_barrier(0);
    auto mid = [&]() {
      __builtin_amdgcn_sched_barrier(0);
      const f32x2 t = ((sp[0] + sp[1]) + (sp[2] + sp[3])) + ((sp[4] + sp[5]) + (sp[6] + sp[7]));
      const float mu = red32(t.x, t.x) * (1.0f / D);
      const float var = fmaxf(red32(t.y, t.y) * (1.0f / D) - mu * mu, 0.f);
      const float rs = __builtin_amdgcn_rsqf(var + EPS);
      const float k0r = GK0 * rs;
      if (lane < 32) *(LAS f32x4*)(lds + L3_ROW + (lw * 32 + lane) * 16) = (f32x4){-mu, k0r, k0r * 0.044715f * rs * rs, __builtin_amdgcn_rcpf(rs)};
      if (lane < 56) *(LAS float*)(lds + L3_COL + (lw * 56 + lane) * 4) = (cp[0] + cp[1]) + (cp[2] + cp[3]);
      WAIT_LGKM0();
    };
    gemm_loader<C3>(lds, (const char*)(x1 + (size_t)pm * 128 * D), 0, (const char*)(w3 + pn * 224), F * 2, nk, (idx + (idx >> 2)) & 3, lw, lane,
                    NoTail(), mid PROF_PASS);
    PROF_END(3);
    return;
  }
  const int wr = wave >> 1, wc = wave & 1, g = lane >> 4, mm = lane & 15;
  auto nfmap = [&](int f) { return f; };
  f32x4 acc[4][7], dum[4], dum1;
#pragma unroll
  for (int m = 0; m < 4; ++m)
#pragma unroll
    for (int f = 0; f < 7; ++f) acc[m][f] = (f32x4){0.f, 0.f, 0.f, 0.f};
  gemm_consumer<C3, nk, false>(lds, wr, wc, lane, acc, nfmap, dum, dum, dum1, NoOp() PROF_PASS);
  f32x4 cw[7];
#pragma unroll
  for (int f = 0; f < 7; ++f) cw[f] = *(LAS f32x4*)(lds + L3_COL + (wc * 112 + f * 16 + 4 * g) * 4);
  const rsrc_t rh = mk_rsrc(h3, S * F * 2);
  const int obase = (pm * (F / 8) + pn * 28 + wc * 14 + (g >> 1)) * 2048 + (wr * 64 + (g & 1) * 16 + mm) * 16;
#pragma unroll
  for (int m = 0; m < 4; m += 2) {
    const f32x4 c0 = *(LAS f32x4*)(lds + L3_ROW + (wr * 64 + m * 16 + mm) * 16), c1 = *(LAS f32x4*)(lds + L3_ROW + (wr * 64 + m * 16 + 16 + mm) * 16);
#pragma unroll
    for (int f = 0; f < 7; ++f) {
      const u32x2 p0 = {pack2(gelu2(fma2(lo2(cw[f]), splat(c0[0]), lo2(acc[m][f])), c0[1], c0[2], c0[3])),
                        pack2(gelu2(fma2(hi2(cw[f]), splat(c0[0]), hi2(acc[m][f])), c0[1], c0[2], c0[3]))};
      const u32x2 p1 = {pack2(gelu2(fma2(lo2(cw[f]), splat(c1[0]), lo2(acc[m + 1][f])), c1[1], c1[2], c1[3])),
                        pack2(gelu2(fma2(hi2(cw[f]), splat(c1[0]), hi2(acc[m + 1][f])), c1[1], c1[2], c1[3]))};
      st16<AUX_H3>(rh, obase + f * 4096 + m * 256, swap_pair(p0, p1));
    }
  }
  PROF_END(3);
}

typedef Cfg<128, 2, 2, 4, 2> C4;
constexpr int LDS4 = C4::NS * C4::STAGE;
struct Tail4 {
  const char* xs;
  const char* x1s;
  const char* ys;
  static constexpr int LEFT = 0;
  static constexpr int sum(int j) { return 4 * (j < 3 ? j : 3); }
  template <int J> DI void issue(LAS char* dst, int lw, int lane) const {
    if constexpr (J == 0) load_row_tile<64>(dst, xs, D * 2, lw, lane);
    if constexpr (J == 1) {
#pragma unroll
      for (int i = 0; i < 4; ++i) glds16(x1s + (lw + 4 * i) * 2048 + lane * 16, dst + (lw + 4 * i) * 1024);
    }
    if constexpr (J == 2) {
#pragma unroll
      for (int i = 0; i < 4; ++i) {
        const int row = (lw + 4 * i) * 8 + (lane >> 3), pos = lane & 7;
        glds16(ys + (size_t)row * (S * 2) + ((pos ^ (((row >> 1) & 3) << 1)) << 4), dst + (lw + 4 * i) * 1024);
      }
    }
  }
};
__global__ __launch_bounds__(512, 2) void k_g4(const bf16_t* __restrict__ h3, const bf16_t* __restrict__ w4, const bf16_t* __restrict__ x,
                                               const bf16_t* __restrict__ x1, const bf16_t* __restrict__ yT, const f32x2* __restrict__ stats1,
                                               unsigned* __restrict__ xch, bf16_t* __restrict__ out) {
  extern __shared__ __attribute__((aligned(16))) char smem[];
  LAS char* lds = (LAS char*)smem;
  PROF_DECL;
  const int wave = __builtin_amdgcn_readfirstlane(threadIdx.x >> 6), lane = threadIdx.x & 63;
  const int xcd = blockIdx.x & 7, idx = blockIdx.x >> 3;
  const int pm = (xcd >> 1) * 4 + (idx & 3), pn = idx >> 2, kh = xcd & 1;
  const int row0 = pm * 128 + kh * 64;
  constexpr int nk = F / 128;
  if (wave >= 4) {
    Tail4 tl{(const char*)(x + (size_t)row0 * D + pn * 128), (const char*)x1 + (pm * (D / 8) + pn * 16) * 2048 + kh * 1024,
             (const char*)(yT + (size_t)pn * 128 * S + row0)};
    gemm_loader<C4>(lds, (const char*)h3 + (size_t)(pm * 2 + kh) * nk * 16384, 0, (const char*)(w4 + (size_t)kh * (F / 2) * D + pn * 128), D * 2, nk,
                    (idx + (idx >> 2)) & 3, wave - 4, lane, tl, NoOp() PROF_PASS);
    PROF_END(4);
    return;
  }
  const int wr = wave >> 1, wc = wave & 1, g = lane >> 4, mm = lane & 15;
  f32x2 st[2];
#pragma unroll
  for (int m = 0; m < 2; ++m) st[m] = stats1[row0 + wr * 32 + m * 16 + mm];
  f32x4 acc[4][4], dum[4], dum1;
#pragma unroll
  for (int m = 0; m < 4; ++m)
#pragma unroll
    for (int f = 0; f < 4; ++f) acc[m][f] = (f32x4){0.f, 0.f, 0.f, 0.f};
  auto nfmap = [&](int f) { return f; };
  gemm_consumer<C4, nk, false>(lds, wr, wc, lane, acc, nfmap, dum, dum, dum1, NoOp() PROF_PASS);
  const rsrc_t rx = mk_rsrc(xch, 256 * 16384);
  f32x4 keep[2][4];
#pragma unroll
  for (int m = 0; m < 2; ++m)
#pragma unroll
    for (int f = 0; f < 4; f += 2) {
      const f32x4 s0 = kh ? acc[m][f] : acc[2 + m][f], s1 = kh ? acc[m][f + 1] : acc[2 + m][f + 1];
      keep[m][f] = kh ? acc[2 + m][f] : acc[m][f];
      keep[m][f + 1] = kh ? acc[2 + m][f + 1] : acc[m][f + 1];
      st16<AUX_SC1>(rx, ((blockIdx.x ^ 1) * 16 + wave * 4 + m * 2 + (f >> 1)) * 1024 + lane * 16,
                    (u32x4){min(pack2(lo2(s0)), XCH_EMPTY - 1), min(pack2(hi2(s0)), XCH_EMPTY - 1), min(pack2(lo2(s1)), XCH_EMPTY - 1),
                            min(pack2(hi2(s1)), XCH_EMPTY - 1)});
    }
  STAMP(4);
  LAS char* xt = lds + tail_buf<C4>(nk, 0) * C4::STAGE;
  LAS char* x1t = lds + tail_buf<C4>(nk, 1) * C4::STAGE;
  LAS char* yt = lds + tail_buf<C4>(nk, 2) * C4::STAGE;
  f32x2 xv[2][4][2], cv[2][4][2];
#pragma unroll
  for (int m = 0; m < 2; ++m) {
    const int rl = wr * 32 + m * 16 + mm;
    const f32x2 r = splat(st[m].y), nmr = splat(-st[m].x * st[m].y);
#pragma unroll
    for (int f = 0; f < 4; ++f) {
      const u32x2 xw = read_row_tile(xt, rl, wc * 64 + f * 16 + 4 * g);
      const u32x2 x1w = *(LAS u32x2*)(x1t + (wc * 8 + f * 2 + (g >> 1)) * 1024 + rl * 16 + (g & 1) * 8);
      const int yrow = wc * 64 + f * 16 + 4 * g + (mm >> 2);
      const u32x2 yw = __builtin_bit_cast(u32x2, tr_read(yt + yrow * 128 + (((wr * 2 + m) ^ ((yrow >> 1) & 3)) << 5) + 8 * (mm & 3)));
      xv[m][f][0] = bf_pair(xw.x), xv[m][f][1] = bf_pair(xw.y);
      cv[m][f][0] = fma2(xv[m][f][0], r, nmr) * bf_pair(yw.x), cv[m][f][1] = fma2(xv[m][f][1], r, nmr) * bf_pair(yw.y);
      keep[m][f] += (f32x4){bf_lo(x1w.x), bf_hi(x1w.x), bf_lo(x1w.y), bf_hi(x1w.y)};
    }
  }
  __builtin_amdgcn_sched_barrier(0);
  u32x4 pw[4];
  for (int spin = 0; spin < (1 << 20); ++spin) {
    unsigned mx = 0;
    asm volatile("" ::: "memory");
#pragma unroll
    for (int i = 0; i < 4; ++i) {
      pw[i] = ld16<AUX_SC1>(rx, (blockIdx.x * 16 + wave * 4 + i) * 1024 + lane * 16);
      mx = max(max(mx, max(pw[i][0], pw[i][1])), max(pw[i][2], pw[i][3]));
    }
    if (__builtin_amdgcn_ballot_w64(mx == XCH_EMPTY) == 0) break;
  }
  STAMP(5);
  LAS char* ot = lds + tail_buf<C4>(nk, 3) * C4::STAGE + wave * 4096;
  const rsrc_t ro = mk_rsrc(out, S * D * 2);
#pragma unroll
  for (int m = 0; m < 2; ++m) {
#pragma unroll
    for (int f = 0; f < 4; ++f) {
      u32x2 o;
#pragma unroll
      for (int h = 0; h < 2; ++h) {
        const f32x2 z = (h ? hi2(keep[m][f]) : lo2(keep[m][f])) + bf_pair(pw[m * 2 + (f >> 1)][(f & 1) * 2 + h]);
        const f32x2 zl = z * splat(-1.4426950408889634f);
        const f32x2 e = (f32x2){__builtin_amdgcn_exp2f(zl.x), __builtin_amdgcn_exp2f(zl.y)} + splat(1.0f);
        o[h] = pack2(fma2(cv[m][f][h] * z, (f32x2){__builtin_amdgcn_rcpf(e.x), __builtin_amdgcn_rcpf(e.y)}, xv[m][f][h]));
      }
      *(LAS u32x2*)(ot + (m * 16 + mm) * 128 + (((f * 2 + (g >> 1)) ^ (mm & 7)) << 4) + (g & 1) * 8) = o;
    }
#pragma unroll
    for (int i = 0; i < 2; ++i) {
      const int rl = m * 16 + i * 8 + (lane >> 3);
      st16<AUX_OUT>(ro, ((row0 + wr * 32 + rl) * D + pn * 128 + wc * 64) * 2 + (lane & 7) * 16,
                    *(LAS u32x4*)(ot + rl * 128 + (((lane & 7) ^ (rl & 7)) << 4)));
    }
  }
  PROF_END(4);
}

extern "C" void kernel_launch(void* const* d_in, const int* in_sizes, int n_in, void* d_out, int out_size, void* d_ws, size_t ws_size,
                              hipStream_t stream) {
  const bf16_t* x = (const bf16_t*)d_in[0];
  const bf16_t* kk = (const bf16_t*)d_in[1];
  const bf16_t* vv = (const bf16_t*)d_in[2];
  const bf16_t* shk = (const bf16_t*)d_in[3];
  const bf16_t* ssk = (const bf16_t*)d_in[4];
  const bf16_t* ssd = (const bf16_t*)d_in[5];
  const bf16_t* w1 = (const bf16_t*)d_in[7];
  const bf16_t* w2 = (const bf16_t*)d_in[8];
  const bf16_t* w3 = (const bf16_t*)d_in[9];
  const bf16_t* w4 = (const bf16_t*)d_in[10];
  char* ws = (char*)d_ws;
  constexpr size_t SD = (size_t)S * D * 2;
  bf16_t* uT = (bf16_t*)(ws);
  bf16_t* hs = (bf16_t*)(ws + SD);
  bf16_t* yT = (bf16_t*)(ws + 2 * SD);
  bf16_t* x1 = (bf16_t*)(ws + 3 * SD);
  bf16_t* h3 = (bf16_t*)(ws + 4 * SD);
  unsigned* xch = (unsigned*)(ws + 4 * SD + (size_t)S * F * 2);
  char* tail = ws + 5 * SD + (size_t)S * F * 2;
  f32x2* stats1 = (f32x2*)tail;
  f32x2* stats2 = (f32x2*)(tail + S * 8);
  float* colsum3 = (float*)(tail + S * 8 + S * 128);
  (void)hipFuncSetAttribute((const void*)k_g1, hipFuncAttributeMaxDynamicSharedMemorySize, LDS1);
  (void)hipFuncSetAttribute((const void*)k_g2, hipFuncAttributeMaxDynamicSharedMemorySize, LDS2);
  (void)hipFuncSetAttribute((const void*)k_g3, hipFuncAttributeMaxDynamicSharedMemorySize, LDS3);
  (void)hipFuncSetAttribute((const void*)k_g4, hipFuncAttributeMaxDynamicSharedMemorySize, LDS4);
  k_g1<<<256, 512, LDS1, stream>>>(x, w1, w2, kk, vv, shk, hs, uT, stats1);
  k_g2<<<256, 768, LDS2, stream>>>(hs, w2, x, stats1, x1, stats2, uT, ssk, ssd, w3, w4, yT, colsum3, xch);
  k_g3<<<256, 512, LDS3, stream>>>(x1, w3, stats2, colsum3, h3);
  k_g4<<<256, 512, LDS4, stream>>>(h3, w4, x, x1, yT, stats1, xch, (bf16_t*)d_out);
}
```

```cpp
#include <hip/hip_runtime.h>
#include <stdint.h>
#define SPREAD
#define AUX_HS 16
#define AUX_X1 16
#define AUX_H3 16

#define LAS __attribute__((address_space(3)))
#define DI __device__ __forceinline__
typedef unsigned short bf16_t;
typedef __bf16 bf16x8 __attribute__((ext_vector_type(8)));
typedef __bf16 bf16x2 __attribute__((ext_vector_type(2)));
typedef short s16x4 __attribute__((ext_vector_type(4)));
typedef float f32x2 __attribute__((ext_vector_type(2)));
typedef float f32x4 __attribute__((ext_vector_type(4)));
typedef unsigned u32x2 __attribute__((ext_vector_type(2)));
typedef unsigned u32x4 __attribute__((ext_vector_type(4)));
typedef unsigned u32x4u __attribute__((ext_vector_type(4), aligned(2)));
typedef __amdgpu_buffer_rsrc_t rsrc_t;

constexpr int S = 2048, D = 1024, F = 3584;
constexpr float EPS = 1e-5f;
constexpr int AUX_NT = 2, AUX_SC1 = 16;
#define AUX_OUT 0

#define PROF_ARG
#define PROF_PASS
#define PROF_DECL
#define STAMP(i)
#define PROF_END(kid)
DI float bf_lo(unsigned w) { return __uint_as_float(w << 16); }
DI float bf_hi(unsigned w) { return __uint_as_float(w & 0xffff0000u); }
DI f32x2 bf_pair(unsigned w) { return (f32x2){bf_lo(w), bf_hi(w)}; }
DI unsigned pack2(f32x2 v) { return __builtin_bit_cast(unsigned, __builtin_convertvector(v, bf16x2)); }
DI unsigned pack_bf16(float lo, float hi) { return pack2((f32x2){lo, hi}); }
DI f32x2 lo2(f32x4 v) { return __builtin_shufflevector(v, v, 0, 1); }
DI f32x2 hi2(f32x4 v) { return __builtin_shufflevector(v, v, 2, 3); }
DI f32x2 splat(float v) { return (f32x2){v, v}; }
DI f32x2 fma2(f32x2 a, f32x2 b, f32x2 c) { return __builtin_elementwise_fma(a, b, c); }
#define BAR() __builtin_amdgcn_s_barrier()
#define WAIT_LGKM0() asm volatile("s_waitcnt lgkmcnt(0)" ::: "memory")
template <int N> DI void wait_vm() { asm volatile("s_waitcnt vmcnt(%0)" ::"n"(N) : "memory"); }
template <int AUX = 0> DI void glds16(const void* src, LAS char* dst) {
  __builtin_amdgcn_global_load_lds((const unsigned*)src, (LAS unsigned*)dst, 16, 0, AUX);
}
DI rsrc_t mk_rsrc(const void* p, unsigned bytes) { return __builtin_amdgcn_make_buffer_rsrc((void*)p, (short)0, (int)bytes, 0x00020000); }
template <int AUX> DI void st16(rsrc_t r, int off, u32x4 v) { __builtin_amdgcn_raw_buffer_store_b128(v, r, off, 0, AUX); }
template <int AUX> DI u32x4 ld16(rsrc_t r, int off) { return __builtin_amdgcn_raw_buffer_load_b128(r, off, 0, AUX); }
DI s16x4 tr_read(LAS char* p) { return __builtin_amdgcn_ds_read_tr16_b64_v4i16((LAS s16x4*)p); }
DI bf16x8 tr_frag(LAS char* p, int step) {
  const s16x4 lo = tr_read(p), hi = tr_read(p + step);
  return __builtin_bit_cast(bf16x8, __builtin_shufflevector(lo, hi, 0, 1, 2, 3, 4, 5, 6, 7));
}
DI f32x4 mfma16(bf16x8 a, bf16x8 b, f32x4 c) { return __builtin_amdgcn_mfma_f32_16x16x32_bf16(a, b, c, 0, 0, 0); }
DI float red16(float a, float b) {
  const auto r = __builtin_amdgcn_permlane16_swap(__float_as_uint(a), __float_as_uint(b), false, false);
  return __uint_as_float(r[0]) + __uint_as_float(r[1]);
}
DI float red32(float a, float b) {
  const auto r = __builtin_amdgcn_permlane32_swap(__float_as_uint(a), __float_as_uint(b), false, false);
  return __uint_as_float(r[0]) + __uint_as_float(r[1]);
}
DI f32x2 gelu2(f32x2 a, float k0r, float k1r, float ir) {
  const f32x2 z = a * fma2(a * a, splat(k1r), splat(k0r));
  const f32x2 e = {__builtin_amdgcn_exp2f(z.x), __builtin_amdgcn_exp2f(z.y)};
  const f32x2 d = fma2(e, splat(ir), splat(ir));
  return a * (f32x2){__builtin_amdgcn_rcpf(d.x), __builtin_amdgcn_rcpf(d.y)};
}
constexpr float GK0 = -2.0f * 0.7978845608028654f * 1.4426950408889634f;
DI u32x4 swap_pair(u32x2 x, u32x2 y) {
  const auto a = __builtin_amdgcn_permlane16_swap(x.x, y.x, false, false);
  const auto b = __builtin_amdgcn_permlane16_swap(x.y, y.y, false, false);
  return (u32x4){a[0], b[0], a[1], b[1]};
}

template <int BN_, int WR_, int WC_, int NS_, int AM_> struct Cfg {
  static constexpr int BN = BN_, WR = WR_, WC = WC_, NS = NS_, AM = AM_;
  static constexpr int TM = 128 / WR, TN = BN / WC, MF = TM / 16, NF = TN / 16;
  static constexpr int ABYTES = 128 * 64 * 2, BBYTES = 64 * BN * 2, STAGE = ABYTES + BBYTES;
  static constexpr int NB = BN / 32, PER = 4 + NB, RB = BN * 2, CPR = BN / 8;
  static DI int swzB(int k) {
    return BN == 64 ? (((k >> 1) & 1) | (((k >> 3) & 1) << 1)) : BN == 128 ? ((2 * (k & 3)) | ((k >> 3) & 1)) : ((k >> 3) & 1);
  }
};

template <class C, class Tail, int J> DI void loader_tail_iters(LAS char* lds, int& buf, int lw, int lane, const Tail& tail) {
  if constexpr (J < C::NS - 1) {
    wait_vm<(C::NS - 2 - J) * C::PER + Tail::sum(J)>();
    BAR();
    tail.template issue<J>(lds + buf * C::STAGE, lw, lane);
    buf = (buf + 1 == C::NS) ? 0 : buf + 1;
    loader_tail_iters<C, Tail, J + 1>(lds, buf, lw, lane, tail);
  }
}

template <class C, class Tail, class Mid>
DI void gemm_loader(LAS char* lds, const char* Ab, int lda_b, const char* Bb, int ldw_b, int nk, int phi, int lw, int lane, const Tail& tail,
                    const Mid& mid PROF_ARG) {
  const unsigned voffA = C::AM == 0 ? (unsigned)((8 * lw + (lane >> 3)) * lda_b + (((lane & 7) ^ ((4 * lw + (lane >> 4)) & 7)) << 4))
                                    : (unsigned)(lw * 1024 + lane * 16);
  unsigned voffB[C::NB];
#pragma unroll
  for (int i = 0; i < C::NB; ++i) {
    const int L = (lw + 4 * i) * 64 + lane, k = L / C::CPR, pos = L % C::CPR;
    voffB[i] = (unsigned)(k * ldw_b + ((pos ^ (C::swzB(k) << 1)) << 4));
  }
  unsigned done = 0;
  auto issue = [&](int t, int buf) {
    const int kt = ((t & 3) == phi && t + 3 < nk) ? t + 3 : __builtin_ctz(~done);
    done |= 1u << kt;
    const char* a = Ab + (C::AM == 0 ? kt * 128 : kt * 16384);
    const char* b = Bb + (size_t)kt * 64 * ldw_b;
    LAS char* dst = lds + buf * C::STAGE + lw * 1024;
#pragma unroll
    for (int i = 0; i < 4; ++i) glds16(a + (C::AM == 0 ? (size_t)i * 32 * lda_b : (size_t)i * 4096) + voffA, dst + i * 4096);
#pragma unroll
    for (int i = 0; i < C::NB; ++i) glds16(b + voffB[i], dst + C::ABYTES + i * 4096);
  };
#pragma unroll
  for (int s = 0; s < C::NS - 1; ++s) issue(s, s);
  mid();
  int buf = C::NS - 1;
  STAMP(1);
  for (int t = C::NS - 1; t < nk; ++t) {
    wait_vm<(C::NS - 2) * C::PER>();
    BAR();
    issue(t, buf);
    buf = (buf + 1 == C::NS) ? 0 : buf + 1;
  }
  STAMP(2);
  loader_tail_iters<C, Tail, 0>(lds, buf, lw, lane, tail);
  wait_vm<Tail::LEFT>();
  BAR();
}
template <class C> constexpr int tail_buf(int nk, int j) { return (nk + j) % C::NS; }

template <int NM, int NA, int NBF> DI void sched_mix() {
#pragma unroll
  for (int i = 0; i < NA + 2 * NBF; ++i) {
    __builtin_amdgcn_sched_group_barrier(0x8, 1, 0);
    __builtin_amdgcn_sched_group_barrier(0x100, 1, 0);
  }
  __builtin_amdgcn_sched_group_barrier(0x8, NM - NA - 2 * NBF, 0);
  return;
#pragma unroll
  for (int i = 0; i < NA; ++i) {
    __builtin_amdgcn_sched_group_barrier(0x8, 1, 0);
    __builtin_amdgcn_sched_group_barrier(0x100, 1, 0);
  }
#pragma unroll
  for (int i = 0; i < NBF; ++i) {
    __builtin_amdgcn_sched_group_barrier(0x8, 1, 0);
    __builtin_amdgcn_sched_group_barrier(0x100, 2, 0);
  }
  __builtin_amdgcn_sched_group_barrier(0x8, NM - NA - NBF, 0);
}
template <int V> struct IC { static constexpr int v = V; };

template <class C, int NK, bool STATS, class NfMap, class First>
DI void gemm_consumer(LAS char* lds, int wr, int wc, int lane, f32x4 (&acc)[C::MF][C::NF], const NfMap& nfmap, f32x4 (&rsum)[C::MF],
                      f32x4 (&rsq)[C::MF], f32x4& csum, const First& first PROF_ARG) {
  const int g = lane >> 4, q = (lane & 15) >> 2, p = lane & 3;
  const int aoff0 = C::AM == 0 ? (wr * C::TM + (lane & 15)) * 128 + ((g ^ ((lane >> 1) & 7)) << 4)
                               : g * 2048 + (wr * (C::AM == 1 ? C::TM : 32) + (lane & 15)) * 16;
  const int aoff1 = C::AM == 0 ? aoff0 ^ 64 : aoff0 + 8192;
  int boff[C::NF];
#pragma unroll
  for (int f = 0; f < C::NF; ++f) {
    const int k = 8 * g + q;
    boff[f] = C::ABYTES + k * C::RB + (((wc * C::NF + nfmap(f)) ^ C::swzB(k)) << 5) + 8 * p;
  }
  bf16x8 a0[C::MF], b0[C::NF], a1[C::MF], b1[C::NF];
  const bf16x8 ones = __builtin_bit_cast(bf16x8, (u32x4){0x3f803f80u, 0x3f803f80u, 0x3f803f80u, 0x3f803f80u});
  auto ldfr = [&](bf16x8(&a)[C::MF], bf16x8(&b)[C::NF], LAS char* st, int ks) {
#pragma unroll
    for (int m = 0; m < C::MF; ++m)
      a[m] = *(LAS bf16x8*)(st + (ks ? aoff1 : aoff0) + (C::AM == 0 ? m * 2048 : C::AM == 1 ? m * 256 : (m >> 1) * 1024 + (m & 1) * 256));
#pragma unroll
    for (int f = 0; f < C::NF; ++f) b[f] = tr_frag(st + boff[f] + ks * 32 * C::RB, 4 * C::RB);
  };
  auto mma = [&](bf16x8(&a)[C::MF], bf16x8(&b)[C::NF]) {
    if constexpr (STATS) {
#pragma unroll
      for (int m = 0; m < C::MF; ++m) {
        rsum[m] = mfma16(ones, a[m], rsum[m]);
        rsq[m] = mfma16(a[m], a[m], rsq[m]);
      }
      csum = mfma16(b[0], ones, csum);
    }
#pragma unroll
    for (int f = 0; f < C::NF; ++f)
#pragma unroll
      for (int m = 0; m < C::MF; ++m) acc[m][f] = mfma16(b[f], a[m], acc[m][f]);
  };
  constexpr int NM = C::MF * C::NF + (STATS ? 2 * C::MF + 1 : 0);
  auto step = [&](auto slot) {
    constexpr int SL = decltype(slot)::v;
    ldfr(a1, b1, lds + SL * C::STAGE, 1);
    mma(a0, b0);
    sched_mix<NM, C::MF, C::NF>();
    __builtin_amdgcn_sched_barrier(0);
    WAIT_LGKM0();
    BAR();
    asm volatile("" ::: "memory");
    __builtin_amdgcn_sched_barrier(0);
    ldfr(a0, b0, lds + ((SL + 1) % C::NS) * C::STAGE, 0);
    mma(a1, b1);
    sched_mix<NM, C::MF, C::NF>();
    __builtin_amdgcn_sched_barrier(0);
  };
  STAMP(1);
  WAIT_LGKM0();
  BAR();
  asm volatile("" ::: "memory");
  STAMP(2);
  ldfr(a0, b0, lds, 0);
  first();
  __builtin_amdgcn_sched_barrier(0);
#pragma unroll 1
  for (int it = 0; it < NK / C::NS; ++it) {
    step(IC<0>());
    step(IC<1>());
    step(IC<2>());
    if constexpr (C::NS > 3) step(IC<3 % C::NS>());
    if constexpr (C::NS > 4) step(IC<4 % C::NS>());
  }
  static_assert(NK % C::NS <= 1 && C::NS <= 5, "ring");
  if constexpr (NK % C::NS == 1) step(IC<0>());
  STAMP(3);
}

struct NoOp { DI void operator()() const {} };
struct NoTail {
  static constexpr int LEFT = 0;
  static constexpr int sum(int) { return 0; }
  template <int J> DI void issue(LAS char*, int, int) const {}
};
template <int ROWS> DI void load_row_tile(LAS char* dst, const char* src, int ld_b, int lw, int lane) {
#pragma unroll
  for (int i = 0; i < ROWS / 16; ++i) {
    const int row = (lw + 4 * i) * 4 + (lane >> 4), pos = lane & 15;
    glds16(src + (size_t)row * ld_b + ((pos ^ (row & 15)) << 4), dst + (lw + 4 * i) * 1024);
  }
}
DI u32x2 read_row_tile(LAS char* t, int row, int col) {
  return *(LAS u32x2*)(t + row * 256 + (((col >> 3) ^ (row & 15)) << 4) + ((col & 4) << 1));
}

typedef Cfg<64, 4, 1, 5, 0> C1;
constexpr int L1_U = C1::NS * C1::STAGE, L1_CS = L1_U + 16384, LDS1 = L1_CS + 256;
struct Tail1 {
  const char* w;
  static constexpr int LEFT = 8;
  static constexpr int sum(int j) { return j > 0 ? 8 : 0; }
  template <int J> DI void issue(LAS char* dst, int lw, int lane) const {
    if constexpr (J == 0) {
#pragma unroll
      for (int i = 0; i < 8; ++i) glds16(w + (lw * 8 + i) * (D * 2) + lane * 16, dst + lw * 1024);
    }
  }
};
__global__ __launch_bounds__(512, 2) void k_g1(const bf16_t* __restrict__ x, const bf16_t* __restrict__ w1, const bf16_t* __restrict__ w2,
                                               const bf16_t* __restrict__ kk, const bf16_t* __restrict__ vv, const bf16_t* __restrict__ shk,
                                               bf16_t* __restrict__ hs, bf16_t* __restrict__ uT, f32x2* __restrict__ stats1) {
  extern __shared__ __attribute__((aligned(16))) char smem[];
  LAS char* lds = (LAS char*)smem;
  PROF_DECL;
  const int wave = __builtin_amdgcn_readfirstlane(threadIdx.x >> 6), lane = threadIdx.x & 63;
  const int xcd = blockIdx.x & 7, idx = blockIdx.x >> 3;
  const int pm = (xcd >> 1) * 4 + (idx & 3), pn = (xcd & 1) * 8 + (idx >> 2);
  constexpr int nk = D / 64;
  if (wave >= 4) {
    Tail1 tl{(const char*)(w2 + (size_t)idx * 32 * D + (xcd & 1) * 512)};
    gemm_loader<C1>(lds, (const char*)(x + (size_t)pm * 128 * D), D * 2, (const char*)(w1 + pn * 64), D * 2, nk, (idx + (idx >> 2)) & 3, wave - 4,
                    lane, tl, NoOp() PROF_PASS);
    BAR();
    wait_vm<0>();
    PROF_END(1);
    return;
  }
  const int wr = wave;
  {
    const int tq = lane >> 3, dq = lane & 7, t0 = pm * 128 + wr * 32 + tq * 4, d0 = pn * 64 + dq * 8;
    const rsrc_t rk = mk_rsrc(kk, S * D * 2), rv = mk_rsrc(vv, S * D * 2);
    u32x4 kr[7], vr[4], sk[4];
#pragma unroll
    for (int j = 0; j < 4; ++j) sk[j] = *(const u32x4*)(shk + d0 * 4 + j * 8);
#pragma unroll
    for (int j = 0; j < 7; ++j) kr[j] = ld16<AUX_NT>(rk, (max(t0 - 3 + j, 0) * D + d0) * 2);
#pragma unroll
    for (int j = 0; j < 4; ++j) vr[j] = ld16<AUX_NT>(rv, ((t0 + j) * D + d0) * 2);
    if (t0 == 0) kr[0] = kr[1] = kr[2] = (u32x4){0u, 0u, 0u, 0u};
    LAS char* ub = lds + L1_U + dq * 2048 + (((15 - 4 * wr - (tq >> 1)) ^ dq) << 4) + ((~tq & 1) << 3);
#pragma unroll
    for (int c = 0; c < 4; ++c) {
      const f32x2 s0 = {bf_lo(sk[c][0]), bf_lo(sk[c][2])}, s1 = {bf_hi(sk[c][0]), bf_hi(sk[c][2])};
      const f32x2 s2 = {bf_lo(sk[c][1]), bf_lo(sk[c][3])}, s3 = {bf_hi(sk[c][1]), bf_hi(sk[c][3])};
      f32x2 kf[7], u[4];
#pragma unroll
      for (int j = 0; j < 7; ++j) kf[j] = bf_pair(kr[j][c]);
#pragma unroll
      for (int j = 0; j < 4; ++j) u[j] = fma2(kf[j + 3], s0, fma2(kf[j + 2], s1, fma2(kf[j + 1], s2, kf[j] * s3))) * bf_pair(vr[j][c]);
      *(LAS u32x2*)(ub + (2 * c) * 256) = (u32x2){pack_bf16(u[3].x, u[2].x), pack_bf16(u[1].x, u[0].x)};
      *(LAS u32x2*)(ub + (2 * c + 1) * 256) = (u32x2){pack_bf16(u[3].y, u[2].y), pack_bf16(u[1].y, u[0].y)};
    }
  }
  f32x4 acc[2][4], rsum[2], rsq[2], csum = {0.f, 0.f, 0.f, 0.f};
#pragma unroll
  for (int m = 0; m < 2; ++m) {
    rsum[m] = rsq[m] = (f32x4){0.f, 0.f, 0.f, 0.f};
#pragma unroll
    for (int f = 0; f < 4; ++f) acc[m][f] = (f32x4){0.f, 0.f, 0.f, 0.f};
  }
  auto nfmap = [&](int f) { return f ^ wr; };
  auto first = [&]() {
    const rsrc_t ru = mk_rsrc(uT, S * D * 2);
#pragma unroll
    for (int i = 0; i < 4; ++i) {
      const int d = wr * 16 + i * 4 + (lane >> 4);
      st16<AUX_SC1>(ru, ((pn * 64 + d) * S + (S - 128 - pm * 128)) * 2 + (lane & 15) * 16,
                    *(LAS u32x4*)(lds + L1_U + d * 256 + (((lane & 15) ^ (d >> 3)) << 4)));
    }
  };
  gemm_consumer<C1, nk, true>(lds, wr, 0, lane, acc, nfmap, rsum, rsq, csum, first PROF_PASS);
  const int g = lane >> 4, mm = lane & 15;
  LAS float* cs = (LAS float*)(lds + L1_CS);
  if (mm == 0) *(LAS f32x4*)(cs + wr * 16 + 4 * g) = csum;
  WAIT_LGKM0();
  BAR();
  asm volatile("" ::: "memory");
  f32x4 cw[4];
#pragma unroll
  for (int f = 0; f < 4; ++f) cw[f] = *(LAS f32x4*)(cs + (f ^ wr) * 16 + 4 * g);
  const int grp = pn >> 2, delta = (grp & 1) ? -((grp >> 1) + 1) : ((grp >> 1) + 1);
  const rsrc_t rh = mk_rsrc(hs, S * D * 2);
#pragma unroll
  for (int m = 0; m < 2; ++m) {
    const int sel = mm & 3;
    float dq = sel == 0 ? rsq[m][0] : sel == 1 ? rsq[m][1] : sel == 2 ? rsq[m][2] : rsq[m][3];
    dq = __shfl(dq, mm + 16 * (mm >> 2));
    const float mu = rsum[m][0] * (1.0f / D);
    const float var = fmaxf(dq * (1.0f / D) - mu * mu, 0.f);
    const float r = __builtin_amdgcn_rsqf(var + EPS);
    const float rr = r * __builtin_amdgcn_rsqf(var * r * r + EPS);
    const int row = pm * 128 + wr * 32 + m * 16 + mm;
    if (pn == 0 && g == 0) stats1[row] = (f32x2){mu, r};
    const float k0r = GK0 * rr, k1r = k0r * 0.044715f * rr * rr, ir = __builtin_amdgcn_rcpf(rr);
    u32x2 pk[4];
#pragma unroll
    for (int f = 0; f < 4; ++f)
      pk[f] = (u32x2){pack2(gelu2(fma2(lo2(cw[f]), splat(-mu), lo2(acc[m][f])), k0r, k1r, ir)),
                      pack2(gelu2(fma2(hi2(cw[f]), splat(-mu), hi2(acc[m][f])), k0r, k1r, ir))};
    const int drow = (row + delta) & (S - 1);
    const bool wrap = drow != row + delta;
#pragma unroll
    for (int f = 0; f < 4; f += 2) {
      u32x4 o = swap_pair(pk[f], pk[f + 1]);
      if (wrap) o = (u32x4){0u, 0u, 0u, 0u};
      const int chunk = pn * 8 + (((f + (g & 1)) ^ wr) << 1) + (g >> 1);
      st16<AUX_HS>(rh, ((drow >> 7) * (D / 8) + chunk) * 2048 + (drow & 127) * 16, o);
    }
  }
  PROF_END(1);
}

typedef Cfg<128, 2, 2, 4, 1> C2;
struct Tail2 {
  const char* xs;
  static constexpr int LEFT = 0;
  static constexpr int sum(int j) { return j > 0 ? 8 : 0; }
  template <int J> DI void issue(LAS char* dst, int lw, int lane) const {
    if constexpr (J == 0) load_row_tile<128>(dst, xs, D * 2, lw, lane);
  }
};
constexpr int CV_K = 4608, CV_U = 4160, CV_W = CV_K + 2 * CV_U;
constexpr int L2_RED = 8 * CV_W, L2_DUMMY = L2_RED + 2048;
constexpr int LDS2 = C2::NS * C2::STAGE;
static_assert(L2_DUMMY + 4096 + 16 <= LDS2, "conv LDS");
constexpr unsigned XCH_EMPTY = 0xffffffffu;

DI void conv_wave(LAS char* lds, int d, int wave, int lane, const bf16_t* __restrict__ uT, const bf16_t* __restrict__ ssk,
                  const bf16_t* __restrict__ ssd, bf16_t* __restrict__ yT PROF_ARG) {
  LAS char* base = lds + wave * CV_W;
  const char* ksrc = (const char*)(ssk + (size_t)d * S);
  const char* usrc = (const char*)(uT + (size_t)d * S);
  u32x4 sh[4];
#pragma unroll
  for (int i = 0; i < 4; ++i) glds16<AUX_NT>(ksrc + i * 1024 + lane * 16, base + 512 + i * 1024);
#pragma unroll
  for (int i = 0; i < 4; ++i) glds16(usrc + i * 1024 + lane * 16, base + CV_K + i * 1024);
#pragma unroll
  for (int i = 0; i < 4; ++i) sh[i] = *(const u32x4u*)(usrc + i * 1024 + lane * 16 + 2);
  const float dcoef = bf_lo(ssd[d]);
  BAR();
  *(LAS u32x2*)(base + lane * 8) = (u32x2){0u, 0u};
  if (lane < 32) *(LAS unsigned*)(base + CV_K + 4096 + (lane >> 4) * CV_U + (lane & 15) * 4) = 0u;
  if (lane == 63) sh[3][3] &= 0xffffu;
#pragma unroll
  for (int i = 0; i < 4; ++i) *(LAS u32x4*)(base + CV_K + CV_U + i * 1024 + lane * 16) = sh[i];
  wait_vm<0>();
  WAIT_LGKM0();
  STAMP(2);
  const int i16 = lane & 15, g = lane >> 4;
  LAS char* tb = base + CV_K + ((i16 & 1) ? 0 : CV_U - 2) + 2 * (2031 + 8 * g - i16) - 512 * 7;
  LAS char* rb = base + 480 + 32 * i16 + 16 * g;
  f32x4 acc[8];
#pragma unroll
  for (int j = 0; j < 8; ++j) acc[j] = (f32x4){0.f, 0.f, 0.f, 0.f};
#pragma unroll 2
  for (int r = 0; r < 8; ++r) {
    bf16x8 T[8], R[8];
#pragma unroll
    for (int a = 0; a < 8; ++a) {
      LAS unsigned* tp = (LAS unsigned*)(tb + 512 * (7 - a) - 64 * r);
      T[a] = __builtin_bit_cast(bf16x8, (u32x4){tp[0], tp[1], tp[2], tp[3]});
      R[a] = *(LAS bf16x8*)(rb + 512 * a - 64 * r);
    }
#pragma unroll
    for (int a = 0; a < 8; ++a)
#pragma unroll
      for (int e = 0; e + a < 8; ++e) acc[a + e] = mfma16(T[a], R[e], acc[a + e]);
  }
  STAMP(3);
#pragma unroll
  for (int jb = 0; jb < 8; ++jb) {
    const int t = 256 * jb + 16 * i16 + 4 * g;
    const u32x2 uw = *(LAS u32x2*)(base + CV_K + 2 * (2044 - t));
    const float y0 = acc[jb][0] + bf_hi(uw.y) * dcoef, y1 = acc[jb][1] + bf_lo(uw.y) * dcoef;
    const float y2 = acc[jb][2] + bf_hi(uw.x) * dcoef, y3 = acc[jb][3] + bf_lo(uw.x) * dcoef;
    *(LAS u32x2*)(base + 2 * t) = (u32x2){pack_bf16(y0, y1), pack_bf16(y2, y3)};
  }
  const rsrc_t ry = mk_rsrc(yT, S * D * 2);
#pragma unroll
  for (int i = 0; i < 4; ++i) st16<AUX_SC1>(ry, d * (S * 2) + i * 1024 + lane * 16, *(LAS u32x4*)(base + i * 1024 + lane * 16));
}

DI void aux_wave(LAS char* lds, int xcd, int ci, int aw, int lane, const bf16_t* __restrict__ w3, const bf16_t* __restrict__ w4,
                 float* __restrict__ colsum3, unsigned* __restrict__ xch) {
  const int cb = xcd * 16 + ci;
  LAS unsigned* cnt = (LAS unsigned*)(lds + L2_DUMMY + 4096);
  if (aw == 0) *cnt = 0u;
  WAIT_LGKM0();
  BAR();
  u32x4 wv[16];
  const int cc = ci < 14 ? ci : 0;
#pragma unroll
  for (int i = 0; i < 16; ++i)
    wv[i] = *(const u32x4*)(w3 + (size_t)((xcd >> 1) * 256 + aw * 64 + i * 4 + (lane >> 4)) * F + (xcd & 1) * 1792 + cc * 128 + (lane & 15) * 8);
  __builtin_amdgcn_sched_barrier(0);
#pragma unroll
  for (int i = 0; i < 14; ++i) glds16((const char*)w4 + ((cb * 4 + aw) * 14 + i) * 1024 + lane * 16, lds + L2_DUMMY + aw * 1024);
  const rsrc_t rx = mk_rsrc(xch, 256 * 16384);
#pragma unroll
  for (int i = 0; i < 8; ++i) st16<AUX_SC1>(rx, ((cb * 4 + aw) * 8 + i) * 1024 + lane * 16, (u32x4){XCH_EMPTY, XCH_EMPTY, XCH_EMPTY, XCH_EMPTY});
  __builtin_amdgcn_sched_barrier(0);
  f32x2 cs[4];
#pragma unroll
  for (int c = 0; c < 4; ++c) {
    cs[c] = bf_pair(wv[0][c]);
#pragma unroll
    for (int i = 1; i < 16; ++i) cs[c] += bf_pair(wv[i][c]);
  }
  const float sx = red32(red16(cs[0].x, cs[1].x), red16(cs[2].x, cs[3].x)), sy = red32(red16(cs[0].y, cs[1].y), red16(cs[2].y, cs[3].y));
  *(LAS f32x2*)(lds + L2_RED + aw * 512 + (lane & 15) * 32 + (lane >> 4) * 8) = (f32x2){sx, sy};
  WAIT_LGKM0();
  unsigned old = 0;
  if (lane == 0) old = __hip_atomic_fetch_add(cnt, 1u, __ATOMIC_RELAXED, __HIP_MEMORY_SCOPE_WORKGROUP);
  if (__builtin_amdgcn_readfirstlane(old) == 3 && ci < 14) {
    asm volatile("" ::: "memory");
    LAS f32x2* red = (LAS f32x2*)(lds + L2_RED);
    *(f32x2*)(colsum3 + (xcd >> 1) * F + (xcd & 1) * 1792 + ci * 128 + lane * 2) = (red[lane] + red[64 + lane]) + (red[128 + lane] + red[192 + lane]);
  }
}

__global__ __launch_bounds__(768, 3) void k_g2(const bf16_t* __restrict__ hs, const bf16_t* __restrict__ w2, const bf16_t* __restrict__ x,
                                               const f32x2* __restrict__ stats1, bf16_t* __restrict__ x1, f32x2* __restrict__ stats2,
                                               const bf16_t* __restrict__ uT, const bf16_t* __restrict__ ssk, const bf16_t* __restrict__ ssd,
                                               const bf16_t* __restrict__ w3, const bf16_t* __restrict__ w4, bf16_t* __restrict__ yT,
                                               float* __restrict__ colsum3, unsigned* __restrict__ xch) {
  extern __shared__ __attribute__((aligned(16))) char smem[];
  LAS char* lds = (LAS char*)smem;
  PROF_DECL;
  const int wave = __builtin_amdgcn_readfirstlane(threadIdx.x >> 6), lane = threadIdx.x & 63;
  const int xcd = blockIdx.x & 7, idx = blockIdx.x >> 3;
  if (idx & 1) {
    const int ci = idx >> 1;
    if (wave < 8) {
      conv_wave(lds, (xcd * 16 + ci) * 8 + wave, wave, lane, uT, ssk, ssd, yT PROF_PASS);
      PROF_END(2);
      return;
    }
    aux_wave(lds, xcd, ci, wave - 8, lane, w3, w4, colsum3, xch);
    STAMP(2);
    wait_vm<0>();
    PROF_END(2);
    return;
  }
  if (wave >= 8) return;
  const int jm = (idx >> 1) & 3, jn = idx >> 3;
  const int pm = (xcd >> 1) * 4 + jm, pn = (xcd & 1) * 4 + jn;
  constexpr int nk = D / 64;
  if (wave >= 4) {
    Tail2 tl{(const char*)(x + (size_t)pm * 128 * D + pn * 128)};
    gemm_loader<C2>(lds, (const char*)(hs + (size_t)pm * 128 * D), 0, (const char*)(w2 + pn * 128), D * 2, nk, (jm + jn) & 3, wave - 4, lane, tl,
                    NoOp() PROF_PASS);
    PROF_END(2);
    return;
  }
  const int wr = wave >> 1, wc = wave & 1, g = lane >> 4, mm = lane & 15;
  f32x2 st[4];
#pragma unroll
  for (int m = 0; m < 4; ++m) st[m] = stats1[pm * 128 + wr * 64 + m * 16 + mm];
  f32x4 acc[4][4], dum[4], dum1;
#pragma unroll
  for (int m = 0; m < 4; ++m)
#pragma unroll
    for (int f = 0; f < 4; ++f) acc[m][f] = (f32x4){0.f, 0.f, 0.f, 0.f};
  auto nfmap = [&](int f) { return f; };
  gemm_consumer<C2, nk, false>(lds, wr, wc, lane, acc, nfmap, dum, dum, dum1, NoOp() PROF_PASS);
  LAS char* xt = lds + tail_buf<C2>(nk, 0) * C2::STAGE;
  const rsrc_t r1 = mk_rsrc(x1, S * D * 2);
  float sum[4], sq[4];
#pragma unroll
  for (int m = 0; m < 4; ++m) {
    const int rl = wr * 64 + m * 16 + mm;
    const f32x2 r = splat(st[m].y), nmr = splat(-st[m].x * st[m].y);
    u32x2 pk[4];
    f32x2 s = {0.f, 0.f}, s2 = {0.f, 0.f};
#pragma unroll
    for (int f = 0; f < 4; ++f) {
      const u32x2 xw = read_row_tile(xt, rl, wc * 64 + f * 16 + 4 * g);
      const f32x2 v0 = lo2(acc[m][f]) + fma2(bf_pair(xw.x), r, nmr), v1 = hi2(acc[m][f]) + fma2(bf_pair(xw.y), r, nmr);
      pk[f] = (u32x2){pack2(v0), pack2(v1)};
      s += v0 + v1;
      s2 = fma2(v0, v0, fma2(v1, v1, s2));
    }
#pragma unroll
    for (int f = 0; f < 4; f += 2)
      st16<AUX_X1>(r1, (pm * (D / 8) + pn * 16 + wc * 8 + ((f + (g & 1)) << 1) + (g >> 1)) * 2048 + rl * 16, swap_pair(pk[f], pk[f + 1]));
    sum[m] = s.x + s.y;
    sq[m] = s2.x + s2.y;
  }
  stats2[(pn * 2 + wc) * S + pm * 128 + wr * 64 + lane] =
      (f32x2){red32(red16(sum[0], sum[1]), red16(sum[2], sum[3])), red32(red16(sq[0], sq[1]), red16(sq[2], sq[3]))};
  PROF_END(2);
}

typedef Cfg<224, 2, 2, 3, 1> C3;
constexpr int L3_ROW = C3::NS * C3::STAGE, L3_COL = L3_ROW + 2048, LDS3 = L3_COL + 1024;
__global__ __launch_bounds__(512, 2) void k_g3(const bf16_t* __restrict__ x1, const bf16_t* __restrict__ w3, const f32x2* __restrict__ stats2,
                                               const float* __restrict__ colsum3, bf16_t* __restrict__ h3) {
  extern __shared__ __attribute__((aligned(16))) char smem[];
  LAS char* lds = (LAS char*)smem;
  PROF_DECL;
  const int wave = __builtin_amdgcn_readfirstlane(threadIdx.x >> 6), lane = threadIdx.x & 63;
  const int xcd = blockIdx.x & 7, idx = blockIdx.x >> 3;
  const int pm = (xcd >> 1) * 4 + (idx & 3), pn = (xcd & 1) * 8 + (idx >> 2);
  constexpr int nk = D / 64;
  if (wave >= 4) {
    const int lw = wave - 4;
    f32x2 sp[8];
    float cp[4];
#pragma unroll
    for (int i = 0; i < 8; ++i) sp[i] = stats2[((lane >> 5) * 8 + i) * S + pm * 128 + lw * 32 + (lane & 31)];
#pragma unroll
    for (int i = 0; i < 4; ++i) cp[i] = colsum3[i * F + pn * 224 + lw * 56 + (lane < 56 ? lane : 0)];
    __builtin_amdgcn_sched_barrier(0);
    auto mid = [&]() {
      __builtin_amdgcn_sched_barrier(0);
      const f32x2 t = ((sp[0] + sp[1]) + (sp[2] + sp[3])) + ((sp[4] + sp[5]) + (sp[6] + sp[7]));
      const float mu = red32(t.x, t.x) * (1.0f / D);
      const float var = fmaxf(red32(t.y, t.y) * (1.0f / D) - mu * mu, 0.f);
      const float rs = __builtin_amdgcn_rsqf(var + EPS);
      const float k0r = GK0 * rs;
      if (lane < 32) *(LAS f32x4*)(lds + L3_ROW + (lw * 32 + lane) * 16) = (f32x4){-mu, k0r, k0r * 0.044715f * rs * rs, __builtin_amdgcn_rcpf(rs)};
      if (lane < 56) *(LAS float*)(lds + L3_COL + (lw * 56 + lane) * 4) = (cp[0] + cp[1]) + (cp[2] + cp[3]);
      WAIT_LGKM0();
    };
    gemm_loader<C3>(lds, (const char*)(x1 + (size_t)pm * 128 * D), 0, (const char*)(w3 + pn * 224), F * 2, nk, (idx + (idx >> 2)) & 3, lw, lane,
                    NoTail(), mid PROF_PASS);
    PROF_END(3);
    return;
  }
  const int wr = wave >> 1, wc = wave & 1, g = lane >> 4, mm = lane & 15;
  auto nfmap = [&](int f) { return f; };
  f32x4 acc[4][7], dum[4], dum1;
#pragma unroll
  for (int m = 0; m < 4; ++m)
#pragma unroll
    for (int f = 0; f < 7; ++f) acc[m][f] = (f32x4){0.f, 0.f, 0.f, 0.f};
  gemm_consumer<C3, nk, false>(lds, wr, wc, lane, acc, nfmap, dum, dum, dum1, NoOp() PROF_PASS);
  f32x4 cw[7];
#pragma unroll
  for (int f = 0; f < 7; ++f) cw[f] = *(LAS f32x4*)(lds + L3_COL + (wc * 112 + f * 16 + 4 * g) * 4);
  const rsrc_t rh = mk_rsrc(h3, S * F * 2);
  const int obase = (pm * (F / 8) + pn * 28 + wc * 14 + (g >> 1)) * 2048 + (wr * 64 + (g & 1) * 16 + mm) * 16;
#pragma unroll
  for (int m = 0; m < 4; m += 2) {
    const f32x4 c0 = *(LAS f32x4*)(lds + L3_ROW + (wr * 64 + m * 16 + mm) * 16), c1 = *(LAS f32x4*)(lds + L3_ROW + (wr * 64 + m * 16 + 16 + mm) * 16);
#pragma unroll
    for (int f = 0; f < 7; ++f) {
      const u32x2 p0 = {pack2(gelu2(fma2(lo2(cw[f]), splat(c0[0]), lo2(acc[m][f])), c0[1], c0[2], c0[3])),
                        pack2(gelu2(fma2(hi2(cw[f]), splat(c0[0]), hi2(acc[m][f])), c0[1], c0[2], c0[3]))};
      const u32x2 p1 = {pack2(gelu2(fma2(lo2(cw[f]), splat(c1[0]), lo2(acc[m + 1][f])), c1[1], c1[2], c1[3])),
                        pack2(gelu2(fma2(hi2(cw[f]), splat(c1[0]), hi2(acc[m + 1][f])), c1[1], c1[2], c1[3]))};
      st16<AUX_H3>(rh, obase + f * 4096 + m * 256, swap_pair(p0, p1));
    }
  }
  PROF_END(3);
}

typedef Cfg<128, 2, 2, 4, 2> C4;
constexpr int LDS4 = C4::NS * C4::STAGE;
struct Tail4 {
  const char* xs;
  const char* x1s;
  const char* ys;
  static constexpr int LEFT = 0;
  static constexpr int sum(int j) { return 4 * (j < 3 ? j : 3); }
  template <int J> DI void issue(LAS char* dst, int lw, int lane) const {
    if constexpr (J == 0) load_row_tile<64>(dst, xs, D * 2, lw, lane);
    if constexpr (J == 1) {
#pragma unroll
      for (int i = 0; i < 4; ++i) glds16(x1s + (lw + 4 * i) * 2048 + lane * 16, dst + (lw + 4 * i) * 1024);
    }
    if constexpr (J == 2) {
#pragma unroll
      for (int i = 0; i < 4; ++i) {
        const int row = (lw + 4 * i) * 8 + (lane >> 3), pos = lane & 7;
        glds16(ys + (size_t)row * (S * 2) + ((pos ^ (((row >> 1) & 3) << 1)) << 4), dst + (lw + 4 * i) * 1024);
      }
    }
  }
};
__global__ __launch_bounds__(512, 2) void k_g4(const bf16_t* __restrict__ h3, const bf16_t* __restrict__ w4, const bf16_t* __restrict__ x,
                                               const bf16_t* __restrict__ x1, const bf16_t* __restrict__ yT, const f32x2* __restrict__ stats1,
                                               unsigned* __restrict__ xch, bf16_t* __restrict__ out) {
  extern __shared__ __attribute__((aligned(16))) char smem[];
  LAS char* lds = (LAS char*)smem;
  PROF_DECL;
  const int wave = __builtin_amdgcn_readfirstlane(threadIdx.x >> 6), lane = threadIdx.x & 63;
  const int xcd = blockIdx.x & 7, idx = blockIdx.x >> 3;
  const int pm = (xcd >> 1) * 4 + (idx & 3), pn = idx >> 2, kh = xcd & 1;
  const int row0 = pm * 128 + kh * 64;
  constexpr int nk = F / 128;
  if (wave >= 4) {
    Tail4 tl{(const char*)(x + (size_t)row0 * D + pn * 128), (const char*)x1 + (pm * (D / 8) + pn * 16) * 2048 + kh * 1024,
             (const char*)(yT + (size_t)pn * 128 * S + row0)};
    gemm_loader<C4>(lds, (const char*)h3 + (size_t)(pm * 2 + kh) * nk * 16384, 0, (const char*)(w4 + (size_t)kh * (F / 2) * D + pn * 128), D * 2, nk,
                    (idx + (idx >> 2)) & 3, wave - 4, lane, tl, NoOp() PROF_PASS);
    PROF_END(4);
    return;
  }
  const int wr = wave >> 1, wc = wave & 1, g = lane >> 4, mm = lane & 15;
  f32x2 st[2];
#pragma unroll
  for (int m = 0; m < 2; ++m) st[m] = stats1[row0 + wr * 32 + m * 16 + mm];
  f32x4 acc[4][4], dum[4], dum1;
#pragma unroll
  for (int m = 0; m < 4; ++m)
#pragma unroll
    for (int f = 0; f < 4; ++f) acc[m][f] = (f32x4){0.f, 0.f, 0.f, 0.f};
  auto nfmap = [&](int f) { return f; };
  gemm_consumer<C4, nk, false>(lds, wr, wc, lane, acc, nfmap, dum, dum, dum1, NoOp() PROF_PASS);
  const rsrc_t rx = mk_rsrc(xch, 256 * 16384);
  f32x4 keep[2][4];
#pragma unroll
  for (int m = 0; m < 2; ++m)
#pragma unroll
    for (int f = 0; f < 4; f += 2) {
      const f32x4 s0 = kh ? acc[m][f] : acc[2 + m][f], s1 = kh ? acc[m][f + 1] : acc[2 + m][f + 1];
      keep[m][f] = kh ? acc[2 + m][f] : acc[m][f];
      keep[m][f + 1] = kh ? acc[2 + m][f + 1] : acc[m][f + 1];
      st16<AUX_SC1>(rx, ((blockIdx.x ^ 1) * 16 + wave * 4 + m * 2 + (f >> 1)) * 1024 + lane * 16,
                    (u32x4){min(pack2(lo2(s0)), XCH_EMPTY - 1), min(pack2(hi2(s0)), XCH_EMPTY - 1), min(pack2(lo2(s1)), XCH_EMPTY - 1),
                            min(pack2(hi2(s1)), XCH_EMPTY - 1)});
    }
  STAMP(4);
  LAS char* xt = lds + tail_buf<C4>(nk, 0) * C4::STAGE;
  LAS char* x1t = lds + tail_buf<C4>(nk, 1) * C4::STAGE;
  LAS char* yt = lds + tail_buf<C4>(nk, 2) * C4::STAGE;
  f32x2 xv[2][4][2], cv[2][4][2];
#pragma unroll
  for (int m = 0; m < 2; ++m) {
    const int rl = wr * 32 + m * 16 + mm;
    const f32x2 r = splat(st[m].y), nmr = splat(-st[m].x * st[m].y);
#pragma unroll
    for (int f = 0; f < 4; ++f) {
      const u32x2 xw = read_row_tile(xt, rl, wc * 64 + f * 16 + 4 * g);
      const u32x2 x1w = *(LAS u32x2*)(x1t + (wc * 8 + f * 2 + (g >> 1)) * 1024 + rl * 16 + (g & 1) * 8);
      const int yrow = wc * 64 + f * 16 + 4 * g + (mm >> 2);
      const u32x2 yw = __builtin_bit_cast(u32x2, tr_read(yt + yrow * 128 + (((wr * 2 + m) ^ ((yrow >> 1) & 3)) << 5) + 8 * (mm & 3)));
      xv[m][f][0] = bf_pair(xw.x), xv[m][f][1] = bf_pair(xw.y);
      cv[m][f][0] = fma2(xv[m][f][0], r, nmr) * bf_pair(yw.x), cv[m][f][1] = fma2(xv[m][f][1], r, nmr) * bf_pair(yw.y);
      keep[m][f] += (f32x4){bf_lo(x1w.x), bf_hi(x1w.x), bf_lo(x1w.y), bf_hi(x1w.y)};
    }
  }
  __builtin_amdgcn_sched_barrier(0);
  u32x4 pw[4];
  for (int spin = 0; spin < (1 << 20); ++spin) {
    unsigned mx = 0;
    asm volatile("" ::: "memory");
#pragma unroll
    for (int i = 0; i < 4; ++i) {
      pw[i] = ld16<AUX_SC1>(rx, (blockIdx.x * 16 + wave * 4 + i) * 1024 + lane * 16);
      mx = max(max(mx, max(pw[i][0], pw[i][1])), max(pw[i][2], pw[i][3]));
    }
    if (__builtin_amdgcn_ballot_w64(mx == XCH_EMPTY) == 0) break;
  }
  STAMP(5);
  LAS char* ot = lds + tail_buf<C4>(nk, 3) * C4::STAGE + wave * 4096;
  const rsrc_t ro = mk_rsrc(out, S * D * 2);
#pragma unroll
  for (int m = 0; m < 2; ++m) {
#pragma unroll
    for (int f = 0; f < 4; ++f) {
      u32x2 o;
#pragma unroll
      for (int h = 0; h < 2; ++h) {
        const f32x2 z = (h ? hi2(keep[m][f]) : lo2(keep[m][f])) + bf_pair(pw[m * 2 + (f >> 1)][(f & 1) * 2 + h]);
        const f32x2 zl = z * splat(-1.4426950408889634f);
        const f32x2 e = (f32x2){__builtin_amdgcn_exp2f(zl.x), __builtin_amdgcn_exp2f(zl.y)} + splat(1.0f);
        o[h] = pack2(fma2(cv[m][f][h] * z, (f32x2){__builtin_amdgcn_rcpf(e.x), __builtin_amdgcn_rcpf(e.y)}, xv[m][f][h]));
      }
      *(LAS u32x2*)(ot + (m * 16 + mm) * 128 + (((f * 2 + (g >> 1)) ^ (mm & 7)) << 4) + (g & 1) * 8) = o;
    }
#pragma unroll
    for (int i = 0; i < 2; ++i) {
      const int rl = m * 16 + i * 8 + (lane >> 3);
      st16<AUX_OUT>(ro, ((row0 + wr * 32 + rl) * D + pn * 128 + wc * 64) * 2 + (lane & 7) * 16,
                    *(LAS u32x4*)(ot + rl * 128 + (((lane & 7) ^ (rl & 7)) << 4)));
    }
  }
  PROF_END(4);
}

extern "C" void kernel_launch(void* const* d_in, const int* in_sizes, int n_in, void* d_out, int out_size, void* d_ws, size_t ws_size,
                              hipStream_t stream) {
  const bf16_t* x = (const bf16_t*)d_in[0];
  const bf16_t* kk = (const bf16_t*)d_in[1];
  const bf16_t* vv = (const bf16_t*)d_in[2];
  const bf16_t* shk = (const bf16_t*)d_in[3];
  const bf16_t* ssk = (const bf16_t*)d_in[4];
  const bf16_t* ssd = (const bf16_t*)d_in[5];
  const bf16_t* w1 = (const bf16_t*)d_in[7];
  const bf16_t* w2 = (const bf16_t*)d_in[8];
  const bf16_t* w3 = (const bf16_t*)d_in[9];
  const bf16_t* w4 = (const bf16_t*)d_in[10];
  char* ws = (char*)d_ws;
  constexpr size_t SD = (size_t)S * D * 2;
  bf16_t* uT = (bf16_t*)(ws);
  bf16_t* hs = (bf16_t*)(ws + SD);
  bf16_t* yT = (bf16_t*)(ws + 2 * SD);
  bf16_t* x1 = (bf16_t*)(ws + 3 * SD);
  bf16_t* h3 = (bf16_t*)(ws + 4 * SD);
  unsigned* xch = (unsigned*)(ws + 4 * SD + (size_t)S * F * 2);
  char* tail = ws + 5 * SD + (size_t)S * F * 2;
  f32x2* stats1 = (f32x2*)tail;
  f32x2* stats2 = (f32x2*)(tail + S * 8);
  float* colsum3 = (float*)(tail + S * 8 + S * 128);
  (void)hipFuncSetAttribute((const void*)k_g1, hipFuncAttributeMaxDynamicSharedMemorySize, LDS1);
  (void)hipFuncSetAttribute((const void*)k_g2, hipFuncAttributeMaxDynamicSharedMemorySize, LDS2);
  (void)hipFuncSetAttribute((const void*)k_g3, hipFuncAttributeMaxDynamicSharedMemorySize, LDS3);
  (void)hipFuncSetAttribute((const void*)k_g4, hipFuncAttributeMaxDynamicSharedMemorySize, LDS4);
  k_g1<<<256, 512, LDS1, stream>>>(x, w1, w2, kk, vv, shk, hs, uT, stats1);
  k_g2<<<256, 768, LDS2, stream>>>(hs, w2, x, stats1, x1, stats2, uT, ssk, ssd, w3, w4, yT, colsum3, xch);
  k_g3<<<256, 512, LDS3, stream>>>(x1, w3, stats2, colsum3, h3);
  k_g4<<<256, 512, LDS4, stream>>>(h3, w4, x, x1, yT, stats1, xch, (bf16_t*)d_out);
}
```

```cpp
#include <hip/hip_runtime.h>
#include <stdint.h>

#define LAS __attribute__((address_space(3)))
#define DI __device__ __forceinline__
typedef unsigned short bf16_t;
typedef __bf16 bf16x8 __attribute__((ext_vector_type(8)));
typedef __bf16 bf16x2 __attribute__((ext_vector_type(2)));
typedef short s16x4 __attribute__((ext_vector_type(4)));
typedef float f32x2 __attribute__((ext_vector_type(2)));
typedef float f32x4 __attribute__((ext_vector_type(4)));
typedef float f32x16 __attribute__((ext_vector_type(16)));
typedef float f32x32 __attribute__((ext_vector_type(32)));
typedef unsigned u32x2 __attribute__((ext_vector_type(2)));
typedef unsigned u32x4 __attribute__((ext_vector_type(4)));
typedef __amdgpu_buffer_rsrc_t rsrc_t;

constexpr int S = 2048, D = 1024, F = 3584;
constexpr float EPS = 1e-5f;
constexpr int AUX_NT = 2, AUX_SC1 = 16;
constexpr unsigned WS_U = 0, WS_HS = 4u << 20, WS_Y = 37u << 20, WS_X1 = 12u << 20, WS_H3 = 16u << 20, WS_XCH = 32u << 20, WS_ST = 36u << 20,
                   WS_CS = WS_ST + (256u << 10), WS_P = 41u << 20, WS_EP = WS_P + 8 * 32768, WS_END = WS_EP + 256;
constexpr unsigned FS = 128;
constexpr unsigned LV_U = 1, LV_HS = 2, LV_X1 = 3, LV_Y = 4, LV_L0 = 5, LV_L1 = 6, LV_L2 = 7;
constexpr unsigned HC = 136 * 16;
constexpr unsigned XCH_EMPTY = 0xffffffffu;
constexpr int SPIN = 1 << 16;
constexpr unsigned CODE_BYTES = 37888;
constexpr unsigned SL1 = 25856, SL1_B = 17408, SL3 = 22560, SL4 = 16640;
constexpr unsigned CV = 109184, CVW = 12832, CV_K0 = 4576, CV_K1 = 8736;
constexpr unsigned L3_ROW = 7 * SL3, L3_COL = L3_ROW + 1024;
constexpr unsigned L_ST1 = CV + 4 * CVW, L_CS1 = L_ST1 + 1024, L_LSCR = L_CS1 + 256, L_CNT = L_LSCR + 128, L_Y = 8 * SL4, LDS_BYTES = 163840;
static_assert(L_CNT + 384 <= LDS_BYTES && L3_COL + 896 <= L_ST1 && 4 * SL1 <= CV, "LDS map");

#define PROF_DECL
#define STAMP(i)
#define PROF_END()
#define HB_DECL
#define HB_OUT
#define HB_IN
#define HB_CLOB
#define HB() BAR()
#define VB(n) vbar<n>()
#define HB_END()

#define XSTAMP(i)
template <class T> DI LAS T* L(unsigned a) { return (LAS T*)(uintptr_t)a; }
DI float bf_lo(unsigned w) { return __uint_as_float(w << 16); }
DI float bf_hi(unsigned w) { return __uint_as_float(w & 0xffff0000u); }
DI f32x2 bf_pair(unsigned w) { return (f32x2){bf_lo(w), bf_hi(w)}; }
DI unsigned pack2(f32x2 v) { return __builtin_bit_cast(unsigned, __builtin_convertvector(v, bf16x2)); }
DI unsigned pack_bf16(float lo, float hi) { return pack2((f32x2){lo, hi}); }
DI f32x2 lo2(f32x4 v) { return __builtin_shufflevector(v, v, 0, 1); }
DI f32x2 hi2(f32x4 v) { return __builtin_shufflevector(v, v, 2, 3); }
DI f32x2 splat(float v) { return (f32x2){v, v}; }
DI f32x2 fma2(f32x2 a, f32x2 b, f32x2 c) { return __builtin_elementwise_fma(a, b, c); }
template <class V> DI f32x2 pair_at(const V& v, int i) { return (f32x2){v[i], v[i + 1]}; }
#define FENCE() do { asm volatile("" ::: "memory"); __builtin_amdgcn_sched_barrier(0); } while (0)
#define BAR() asm volatile("s_barrier" ::: "memory")
#define LBAR() asm volatile("s_waitcnt lgkmcnt(0)\n\ts_barrier" ::: "memory")
template <int N> DI void wait_vm() { asm volatile("s_waitcnt vmcnt(%0)" ::"n"(N) : "memory"); }
template <int N> DI void vbar() { asm volatile("s_waitcnt vmcnt(%0)\n\ts_barrier" ::"n"(N) : "memory"); }
DI rsrc_t mk_rsrc(const void* p, unsigned bytes) { return __builtin_amdgcn_make_buffer_rsrc((void*)p, (short)0, (int)bytes, 0x00020000); }
DI u32x4 mk_srd(const void* p, unsigned bytes) { return (u32x4){(unsigned)(uintptr_t)p, (unsigned)((uintptr_t)p >> 32) & 0xffffu, bytes, 0x00020000u}; }
template <int AUX> DI void st16(rsrc_t r, int off, int soff, u32x4 v) { __builtin_amdgcn_raw_buffer_store_b128(v, r, off, soff, AUX); }
template <int AUX> DI u32x4 ld16(rsrc_t r, int off, int soff) { return __builtin_amdgcn_raw_buffer_load_b128(r, off, soff, AUX); }
DI u32x2 tr_read(unsigned a) { return __builtin_bit_cast(u32x2, __builtin_amdgcn_ds_read_tr16_b64_v4i16(L<s16x4>(a))); }
DI f32x4 mfma16(u32x4 a, u32x4 b, f32x4 c) {
  return __builtin_amdgcn_mfma_f32_16x16x32_bf16(__builtin_bit_cast(bf16x8, a), __builtin_bit_cast(bf16x8, b), c, 0, 0, 0);
}
DI float red16(float a, float b) {
  const auto r = __builtin_amdgcn_permlane16_swap(__float_as_uint(a), __float_as_uint(b), false, false);
  return __uint_as_float(r[0]) + __uint_as_float(r[1]);
}
DI float red32(float a, float b) {
  const auto r = __builtin_amdgcn_permlane32_swap(__float_as_uint(a), __float_as_uint(b), false, false);
  return __uint_as_float(r[0]) + __uint_as_float(r[1]);
}
DI f32x2 gelu2(f32x2 a, float k0r, float k1r, float ir) {
  const f32x2 z = a * fma2(a * a, splat(k1r), splat(k0r));
  const f32x2 e = {__builtin_amdgcn_exp2f(z.x), __builtin_amdgcn_exp2f(z.y)};
  const f32x2 d = fma2(e, splat(ir), splat(ir));
  return a * (f32x2){__builtin_amdgcn_rcpf(d.x), __builtin_amdgcn_rcpf(d.y)};
}
constexpr float GK0 = -2.0f * 0.7978845608028654f * 1.4426950408889634f;
DI u32x4 swap_pair(u32x2 x, u32x2 y) {
  const auto a = __builtin_amdgcn_permlane16_swap(x.x, y.x, false, false);
  const auto b = __builtin_amdgcn_permlane16_swap(x.y, y.y, false, false);
  return (u32x4){a[0], b[0], a[1], b[1]};
}

#define DMA_HEAD "s_mov_b32 m0, %0\n\ts_nop 0\n\t"
#define DMA_LD(o, pol) "buffer_load_dwordx4 %1, %2, %3 offen offset:" #o pol " lds\n\t"
#define DMA_ARGS ::"s"(dst), "v"(voff), "s"(srd), "s"(soff) : "memory"
DI void dma1(unsigned dst, unsigned voff, u32x4 srd, unsigned soff) { asm volatile(DMA_HEAD DMA_LD(0, "") DMA_ARGS); }
DI void dma1s(unsigned dst, unsigned voff, u32x4 srd, unsigned soff) { asm volatile(DMA_HEAD DMA_LD(0, " sc1") DMA_ARGS); }
DI void dma2s(unsigned dst, unsigned voff, u32x4 srd, unsigned soff) { asm volatile(DMA_HEAD DMA_LD(0, " sc1") DMA_LD(1024, " sc1") DMA_ARGS); }
DI void dma4s(unsigned dst, unsigned voff, u32x4 srd, unsigned soff) {
  asm volatile(DMA_HEAD DMA_LD(0, " sc1") DMA_LD(1024, " sc1") DMA_LD(2048, " sc1") DMA_LD(3072, " sc1") DMA_ARGS);
}
DI void dma4n(unsigned dst, unsigned voff, u32x4 srd, unsigned soff) {
  asm volatile(DMA_HEAD DMA_LD(0, " nt") DMA_LD(1024, " nt") DMA_LD(2048, " nt") DMA_LD(3072, " nt") DMA_ARGS);
}
DI void dma4p(unsigned dst, const unsigned (&v)[4], u32x4 srd, unsigned soff) {
  asm volatile(DMA_HEAD "buffer_load_dwordx4 %1, %5, %6 offen lds\n\tbuffer_load_dwordx4 %2, %5, %6 offen offset:1056 lds\n\t"
               "buffer_load_dwordx4 %3, %5, %6 offen offset:2112 lds\n\tbuffer_load_dwordx4 %4, %5, %6 offen offset:3168 lds" ::"s"(dst),
               "v"(v[0]), "v"(v[1]), "v"(v[2]), "v"(v[3]), "s"(srd), "s"(soff)
               : "memory");
}
DI void dma_w3(unsigned dst, const unsigned (&v)[4], u32x4 srd, unsigned soff) {
  asm volatile(DMA_HEAD "buffer_load_dwordx4 %1, %5, %6 offen lds\n\tbuffer_load_dwordx4 %2, %5, %6 offen offset:1024 lds\n\t"
               "buffer_load_dwordx4 %3, %5, %6 offen offset:2048 lds\n\ts_mov_b32 exec_hi, 0\n\t"
               "buffer_load_dwordx4 %4, %5, %6 offen offset:3072 lds\n\ts_mov_b32 exec_hi, -1" ::"s"(dst),
               "v"(v[0]), "v"(v[1]), "v"(v[2]), "v"(v[3]), "s"(srd), "s"(soff)
               : "memory");
}
DI void dma_poll(unsigned dst, unsigned voff, u32x4 srd, unsigned soff) {
  asm volatile("s_mov_b32 m0, %0\n\ts_mov_b64 exec, 0x7f\n\tbuffer_load_dword %1, %2, %3 offen sc1 lds\n\ts_mov_b64 exec, -1" DMA_ARGS);
}
DI void touch(unsigned voff, u32x4 srd) {
  asm volatile(DMA_HEAD "buffer_load_dword %1, %2, 0 offen lds" ::"s"(L_CNT + 128), "v"(voff), "s"(srd) : "memory");
}
DI unsigned ld1_async(unsigned voff, u32x4 srd) {
  unsigned r;
  asm volatile("buffer_load_dword %0, %1, %2, 0 offen sc1" : "=&v"(r) : "v"(voff), "s"(srd) : "memory");
  return r;
}
DI u32x4 ld_async(unsigned voff, u32x4 srd, unsigned soff) {
  u32x4 r;
  asm volatile("buffer_load_dwordx4 %0, %1, %2, %3 offen sc1" : "=&v"(r) : "v"(voff), "s"(srd), "s"(soff) : "memory");
  return r;
}
DI constexpr unsigned flag_at(unsigned level, unsigned wg) { return WS_P + level * 32768 + wg * FS; }
DI bool all_eq(unsigned w, unsigned want) { return __builtin_amdgcn_ballot_w64(w != want) == 0; }
DI void spin1(unsigned voff, u32x4 srd, unsigned want) {
#pragma unroll 1
  for (int i = 0; i < SPIN; ++i) {
    unsigned w;
    asm volatile("buffer_load_dword %0, %1, %2, 0 offen sc1\n\ts_waitcnt vmcnt(0)" : "=&v"(w) : "v"(voff), "s"(srd) : "memory");
    if (w == want) voff = WS_END;
    if (all_eq(voff, WS_END)) break;
  }
}
DI unsigned lds_word(unsigned a) { return __builtin_amdgcn_readfirstlane(*(volatile LAS unsigned*)L<unsigned>(a)); }
DI void spin_lds(unsigned a, unsigned want) {
#pragma unroll 1
  for (int i = 0; i < 16 * SPIN && lds_word(a) < want; ++i) __builtin_amdgcn_s_sleep(1);
}
DI bool ready_scr(unsigned scr, unsigned want) { return all_eq(*(volatile LAS unsigned*)L<unsigned>(scr), want); }

DI int relane() {
  int l;
  asm volatile("v_mbcnt_lo_u32_b32 %0, -1, 0\n\tv_mbcnt_hi_u32_b32 %0, -1, %0" : "=v"(l));
  __builtin_assume(l >= 0 && l < 64);
  return l;
}
#define LANES() const int lane = relane(), g = lane >> 4, mm = lane & 15, q = mm >> 2, p = lane & 3

__global__ __launch_bounds__(512, 2) void k_all(const bf16_t* __restrict__ x, const bf16_t* __restrict__ kk, const bf16_t* __restrict__ vv,
                                                const bf16_t* __restrict__ shk, const bf16_t* __restrict__ ssk, const bf16_t* __restrict__ ssd,
                                                const bf16_t* __restrict__ w1, const bf16_t* __restrict__ w2, const bf16_t* __restrict__ w3,
                                                const bf16_t* __restrict__ w4, char* __restrict__ wsp, bf16_t* __restrict__ out) {
  PROF_DECL;
  const int wave = __builtin_amdgcn_readfirstlane(threadIdx.x >> 6);
  const int xcd = blockIdx.x & 7, idx = blockIdx.x >> 3;
  const int pm = (xcd >> 1) * 4 + (idx & 3), kh = xcd & 1, j8 = idx >> 2, pn = kh * 8 + j8;
  unsigned long long pc;
  {
    asm volatile("s_getpc_b64 %0" : "=s"(pc));
    pc &= ~255ull;
    touch(threadIdx.x < 10 ? (threadIdx.x * 32 + idx) * 128 : CODE_BYTES, mk_srd((const void*)pc, CODE_BYTES));
  }
  const u32x4 ws = mk_srd(wsp, WS_END);
  const rsrc_t rw = mk_rsrc(wsp, WS_END);

  if (wave >= 4) {
    const int lw = wave - 4, lane = relane(), g = lane >> 4, mm = lane & 15;
    const unsigned l16 = lane * 16;
    HB_DECL;
    unsigned ep;
    asm volatile("buffer_load_dword %0, off, %1, %2 sc1" : "=&v"(ep) : "s"(ws), "s"(WS_EP) : "memory");
    const unsigned vb64 = (8 * ((lane >> 3 & 1) + 2 * (lane >> 5)) + 4 * (lane >> 4 & 1) + 32 * (lw >> 1) + 2 * (lw & 1)) * 2048 + (lane & 7) * 16 + pn * 128;
    const unsigned vb65 = vb64 + 992;
    auto w64 = [&](u32x4 srd, int kt, unsigned slot) {
      asm volatile(DMA_HEAD "buffer_load_dwordx4 %1, %3, %4 offen lds\n\tbuffer_load_dwordx4 %2, %3, %4 offen offset:1056 lds" ::"s"(slot + SL1_B + lw * 2112),
                   "v"(vb64), "v"(vb65), "s"(srd), "s"(kt * 131072)
                   : "memory");
    };
    const u32x4 sx = mk_srd(x, S * D * 2), s2 = mk_srd(w2, D * D * 2);
    const unsigned cvb = CV + lw * CVW;
    const int d = pn * 64 + pm * 4 + lw;
    const int st2 = 4 * pm + 2 * j8;
    unsigned tag;
    {
      const u32x4 s1 = mk_srd(w1, D * D * 2);
      const unsigned va = (pm * 128 + 16 * (lane >> 3) + 4 * lw) * 2048 + (lane & 7) * 16;
      const unsigned vq[4] = {va, va + 992, va + 2 * 992, va + 3 * 992};
      dma4p(lw * 4224, vq, sx, (st2 & 15) * 128);
      w64(s1, st2 & 15, 0);
      dma4p(SL1 + lw * 4224, vq, sx, ((st2 + 1) & 15) * 128);
#pragma unroll
      for (int s = 1; s < 4; ++s) w64(s1, (st2 + s) & 15, s * SL1);
      if (lane < 60) *L<u32x2>(cvb + 4096 + lane * 8) = (u32x2){0u, 0u};
      if (lane < 16) *L<unsigned>(cvb + CV_K1 - 64 + lane * 4) = 0u;
      if (lane < 4) *L<unsigned>(L_CNT + lane * 4) = 0u;
      STAMP(1);
      asm volatile("s_waitcnt vmcnt(10) lgkmcnt(0)" : "+v"(ep)::"memory");
      HB();
      STAMP(2);
      dma4p(2 * SL1 + lw * 4224, vq, sx, ((st2 + 2) & 15) * 128);
      tag = __builtin_amdgcn_readfirstlane(ep) + 8;
      if (lane < 8) *L<unsigned>(L_LSCR + lane * 4) = tag;
      unsigned m = lw * 4224, ka = ((st2 + 4) & 15) * 128, kb = ka << 10, n = 9;
      VB(8);
      dma4p(3 * SL1 + lw * 4224, vq, sx, ((st2 + 3) & 15) * 128);
      asm volatile(
    "s_mov_b32 m0, %[m]\n"
    "s_nop 0\n"
    "buffer_load_dwordx4 %[a0], %[sa], %[ka] offen offset:0 lds\n"
    "buffer_load_dwordx4 %[a1], %[sa], %[ka] offen offset:1056 lds\n"
    "buffer_load_dwordx4 %[a2], %[sa], %[ka] offen offset:2112 lds\n"
    "buffer_load_dwordx4 %[a3], %[sa], %[ka] offen offset:3168 lds\n"
    "s_add_u32 m0, %[m], %[db]\n"
    "s_add_u32 %[ka], %[ka], 128\n"
    "buffer_load_dwordx4 %[b0], %[sb], %[kb] offen offset:0 lds\n"
    "buffer_load_dwordx4 %[b1], %[sb], %[kb] offen offset:1056 lds\n"
    "s_and_b32 %[ka], %[ka], 2047\n"
    "s_lshl_b32 %[kb], %[ka], 10\n"
    "s_add_u32 %[m], %[m], 25856\n"
    "s_cmp_lt_u32 %[m], 103424\n"
    "s_cselect_b32 %[m], %[m], %[mb]\n"
    "s_mov_b32 m0, %[m]\n"
    "s_waitcnt vmcnt(10)\n"
    "s_barrier\n"
    "buffer_load_dwordx4 %[a0], %[sa], %[ka] offen offset:0 lds\n"
    "buffer_load_dwordx4 %[a1], %[sa], %[ka] offen offset:1056 lds\n"
    "buffer_load_dwordx4 %[a2], %[sa], %[ka] offen offset:2112 lds\n"
    "buffer_load_dwordx4 %[a3], %[sa], %[ka] offen offset:3168 lds\n"
    "s_add_u32 m0, %[m], %[db]\n"
    "s_add_u32 %[ka], %[ka], 128\n"
    "buffer_load_dwordx4 %[b0], %[sb], %[kb] offen offset:0 lds\n"
    "buffer_load_dwordx4 %[b1], %[sb], %[kb] offen offset:1056 lds\n"
    "s_and_b32 %[ka], %[ka], 2047\n"
    "s_lshl_b32 %[kb], %[ka], 10\n"
    "s_add_u32 %[m], %[m], 25856\n"
    "s_cmp_lt_u32 %[m], 103424\n"
    "s_cselect_b32 %[m], %[m], %[mb]\n"
    "s_mov_b32 m0, %[m]\n"
    "1:\n"
    "s_waitcnt vmcnt(12)\n"
    "s_barrier\n"
    "buffer_load_dwordx4 %[a0], %[sa], %[ka] offen offset:0 lds\n"
    "buffer_load_dwordx4 %[a1], %[sa], %[ka] offen offset:1056 lds\n"
    "buffer_load_dwordx4 %[a2], %[sa], %[ka] offen offset:2112 lds\n"
    "buffer_load_dwordx4 %[a3], %[sa], %[ka] offen offset:3168 lds\n"
    "s_add_u32 m0, %[m], %[db]\n"
    "s_add_u32 %[ka], %[ka], 128\n"
    "buffer_load_dwordx4 %[b0], %[sb], %[kb] offen offset:0 lds\n"
    "buffer_load_dwordx4 %[b1], %[sb], %[kb] offen offset:1056 lds\n"
    "s_and_b32 %[ka], %[ka], 2047\n"
    "s_lshl_b32 %[kb], %[ka], 10\n"
    "s_add_u32 %[m], %[m], 25856\n"
    "s_cmp_lt_u32 %[m], 103424\n"
    "s_cselect_b32 %[m], %[m], %[mb]\n"
    "s_mov_b32 m0, %[m]\n"
    "s_sub_u32 %[n], %[n], 1\n"
    "s_cbranch_scc0 1b\n"
          : [m] "+&s"(m), [ka] "+&s"(ka), [kb] "+&s"(kb), [n] "+&s"(n) HB_OUT
          : [a0] "v"(vq[0]), [a1] "v"(vq[1]), [a2] "v"(vq[2]), [a3] "v"(vq[3]), [b0] "v"(vb64), [b1] "v"(vb65), [sa] "s"(sx), [sb] "s"(s1),
            [mb] "s"(lw * 4224), [db] "s"(SL1_B - lw * 2112) HB_IN
          : "memory", "scc", "vcc" HB_CLOB);
      dma4n(cvb + CV_K0, l16, mk_srd(ssk, S * D * 2), d * 4096);
      const unsigned vu = flag_at(LV_U, mm * 16 + pn);
      unsigned pu = ld1_async(vu, ws);
      VB(17);
      w64(s2, st2 & 15, 0);
      VB(13);
      w64(s2, (st2 + 1) & 15, SL1);
      VB(9);
      HB();
      HB();
      asm volatile("s_waitcnt vmcnt(4)" : "+v"(pu)::"memory");
      if (!all_eq(pu, tag + LV_U)) spin1(vu, ws, tag + LV_U);
      STAMP(3);
      dma4s(cvb, l16, ws, WS_U + d * 4096);
      {
        u32x4 kw[4];
        unsigned pv[4];
#pragma unroll
        for (int i = 0; i < 4; ++i) kw[i] = *L<u32x4>(cvb + CV_K0 + i * 1024 + l16), pv[i] = *L<unsigned>(cvb + CV_K0 + i * 1024 + l16 - 4);
#pragma unroll
        for (int i = 0; i < 4; ++i)
          *L<u32x4>(cvb + CV_K1 + i * 1024 + l16) =
              (u32x4){__builtin_amdgcn_alignbit(kw[i][0], pv[i], 16), __builtin_amdgcn_alignbit(kw[i][1], kw[i][0], 16),
                      __builtin_amdgcn_alignbit(kw[i][2], kw[i][1], 16), __builtin_amdgcn_alignbit(kw[i][3], kw[i][2], 16)};
      }
      asm volatile("s_waitcnt vmcnt(0) lgkmcnt(0)" ::: "memory");
      BAR();
    }
    const u32x4 s3 = mk_srd(w3, D * F * 2);
    unsigned vw3[4];
#pragma unroll
    for (int i = 0; i < 4; ++i) {
      const unsigned p = (i * 1024 + l16) % 3584;
      vw3[i] = (8 * lw + p / 448) * (F * 2) + p % 448 - i * 1024 + pn * 448;
    }
    const unsigned b3 = 8192 + 7200 * (lw & 1) + 3584 * (lw >> 1);
    const int st3 = st2 + (pm >> 2 & 2), rd3 = (pm & 4) << 2;
    auto k3b = [&](int h, unsigned slot) { dma_w3(slot + b3, vw3, s3, (((st3 + h) & 15) | rd3) * 32 * (F * 2)); };
    unsigned va3 = WS_X1 + pm * 262144 + lw * 2048 + l16;
    auto k3a = [&](int h, unsigned slot) { dma2s(slot + lw * 2048, va3, ws, (((st3 + h) & 15) | rd3) * 8192); };
    auto gate = [&](unsigned level, unsigned k, unsigned voff, auto&& meanwhile) {
      spin_lds(L_CNT + 8, 4 * k);
      if (lw == 0) {
        if (lane == 0) __builtin_amdgcn_raw_buffer_store_b32(tag + level, rw, 0, flag_at(level, pm * 16 + pn), AUX_SC1);
      } else {
        __builtin_amdgcn_s_sleep(2);
      }
      FENCE();
      meanwhile();
      if (lw == 0) {
        spin1(voff, ws, tag + level);
        *(volatile LAS unsigned*)L<unsigned>(L_CNT + 12) = k;
      } else {
        spin_lds(L_CNT + 12, k);
      }
      FENCE();
    };
    {
      STAMP(4);
      const int nb = (pm + ((lane & 16) ? ((mm & 4) ? 1 : 15) : 0)) & 15;
      unsigned va = WS_HS + pm * 128 * HC + lw * 4096 + l16, va4 = va + 4096;
      asm volatile("" : "+v"(va), "+v"(va4), "+v"(va3), "+v"(vw3[0]), "+v"(vw3[1]), "+v"(vw3[2]), "+v"(vw3[3]));
#define VB3(a, b) do { if (lw != 3) wait_vm<a>(); VB(b); } while (0)
      auto g2a = [&](int s) {
        dma4s(s * SL1 + lw * 4096, va, ws, ((st2 + s) & 15) * 8 * HC);
        if (lw == 3) dma1s(s * SL1 + 16384, va4, ws, ((st2 + s) & 15) * 8 * HC);
      };
      gate(LV_HS, 1, lane < 32 && lane != pn ? flag_at(LV_HS, nb * 16 + mm) : WS_END, [&] {
        w64(s2, (st2 + 2) & 15, 2 * SL1);
        w64(s2, (st2 + 3) & 15, 3 * SL1);
      });
      STAMP(5);
      g2a(0);
      g2a(1);
      unsigned m = lw * 4096, ka = ((st2 + 4) & 15) * 8 * HC, kb = ((st2 + 4) & 15) << 17, n = 9;
      VB3(4, 5);
      STAMP(6);
      g2a(2);
      VB3(4, 5);
      g2a(3);
      asm volatile(
    "s_mov_b32 m0, %[m]\n"
    "s_nop 0\n"
    "buffer_load_dwordx4 %[a0], %[sa], %[ka] offen offset:0 sc1 lds\n"
    "buffer_load_dwordx4 %[a0], %[sa], %[ka] offen offset:1024 sc1 lds\n"
    "buffer_load_dwordx4 %[a0], %[sa], %[ka] offen offset:2048 sc1 lds\n"
    "buffer_load_dwordx4 %[a0], %[sa], %[ka] offen offset:3072 sc1 lds\n"
    "s_add_u32 m0, %[m], 4096\n"
    "s_cmp_lg_u32 %[lw], 3\n"
    "s_cbranch_scc1 2f\n"
    "buffer_load_dwordx4 %[a4], %[sa], %[ka] offen offset:0 sc1 lds\n"
    "2:\n"
    "s_add_u32 m0, %[m], %[db]\n"
    "s_nop 0\n"
    "buffer_load_dwordx4 %[b0], %[sb], %[kb] offen offset:0 lds\n"
    "buffer_load_dwordx4 %[b1], %[sb], %[kb] offen offset:1056 lds\n"
    "s_add_u32 %[kb], %[kb], 0x20000\n"
    "s_and_b32 %[kb], %[kb], 0x1fffff\n"
    "s_lshr_b32 %[ka], %[kb], 17\n"
    "s_mul_i32 %[ka], %[ka], 17408\n"
    "s_add_u32 %[m], %[m], 25856\n"
    "s_cmp_lt_u32 %[m], 103424\n"
    "s_cselect_b32 %[m], %[m], %[mb]\n"
    "s_mov_b32 m0, %[m]\n"
    "s_cmp_eq_u32 %[lw], 3\n"
    "s_cbranch_scc1 3f\n"
    "s_waitcnt vmcnt(10)\n"
    "3:\n"
    "s_waitcnt vmcnt(12)\n"
    "s_barrier\n"
    "buffer_load_dwordx4 %[a0], %[sa], %[ka] offen offset:0 sc1 lds\n"
    "buffer_load_dwordx4 %[a0], %[sa], %[ka] offen offset:1024 sc1 lds\n"
    "buffer_load_dwordx4 %[a0], %[sa], %[ka] offen offset:2048 sc1 lds\n"
    "buffer_load_dwordx4 %[a0], %[sa], %[ka] offen offset:3072 sc1 lds\n"
    "s_add_u32 m0, %[m], 4096\n"
    "s_cmp_lg_u32 %[lw], 3\n"
    "s_cbranch_scc1 2f\n"
    "buffer_load_dwordx4 %[a4], %[sa], %[ka] offen offset:0 sc1 lds\n"
    "2:\n"
    "s_add_u32 m0, %[m], %[db]\n"
    "s_nop 0\n"
    "buffer_load_dwordx4 %[b0], %[sb], %[kb] offen offset:0 lds\n"
    "buffer_load_dwordx4 %[b1], %[sb], %[kb] offen offset:1056 lds\n"
    "s_add_u32 %[kb], %[kb], 0x20000\n"
    "s_and_b32 %[kb], %[kb], 0x1fffff\n"
    "s_lshr_b32 %[ka], %[kb], 17\n"
    "s_mul_i32 %[ka], %[ka], 17408\n"
    "s_add_u32 %[m], %[m], 25856\n"
    "s_cmp_lt_u32 %[m], 103424\n"
    "s_cselect_b32 %[m], %[m], %[mb]\n"
    "s_mov_b32 m0, %[m]\n"
    "1:\n"
    "s_cmp_eq_u32 %[lw], 3\n"
    "s_cbranch_scc1 3f\n"
    "s_waitcnt vmcnt(12)\n"
    "3:\n"
    "s_waitcnt vmcnt(14)\n"
    "s_barrier\n"
    "buffer_load_dwordx4 %[a0], %[sa], %[ka] offen offset:0 sc1 lds\n"
    "buffer_load_dwordx4 %[a0], %[sa], %[ka] offen offset:1024 sc1 lds\n"
    "buffer_load_dwordx4 %[a0], %[sa], %[ka] offen offset:2048 sc1 lds\n"
    "buffer_load_dwordx4 %[a0], %[sa], %[ka] offen offset:3072 sc1 lds\n"
    "s_add_u32 m0, %[m], 4096\n"
    "s_cmp_lg_u32 %[lw], 3\n"
    "s_cbranch_scc1 2f\n"
    "buffer_load_dwordx4 %[a4], %[sa], %[ka] offen offset:0 sc1 lds\n"
    "2:\n"
    "s_add_u32 m0, %[m], %[db]\n"
    "s_nop 0\n"
    "buffer_load_dwordx4 %[b0], %[sb], %[kb] offen offset:0 lds\n"
    "buffer_load_dwordx4 %[b1], %[sb], %[kb] offen offset:1056 lds\n"
    "s_add_u32 %[kb], %[kb], 0x20000\n"
    "s_and_b32 %[kb], %[kb], 0x1fffff\n"
    "s_lshr_b32 %[ka], %[kb], 17\n"
    "s_mul_i32 %[ka], %[ka], 17408\n"
    "s_add_u32 %[m], %[m], 25856\n"
    "s_cmp_lt_u32 %[m], 103424\n"
    "s_cselect_b32 %[m], %[m], %[mb]\n"
    "s_mov_b32 m0, %[m]\n"
    "s_sub_u32 %[n], %[n], 1\n"
    "s_cbranch_scc0 1b\n"
          : [m] "+&s"(m), [ka] "+&s"(ka), [kb] "+&s"(kb), [n] "+&s"(n) HB_OUT
          : [a0] "v"(va), [a4] "v"(va4), [lw] "s"(lw), [b0] "v"(vb64), [b1] "v"(vb65), [sa] "s"(ws), [sb] "s"(s2), [mb] "s"(lw * 4096), [db] "s"(SL1_B - lw * 1984) HB_IN
          : "memory", "scc", "vcc" HB_CLOB);
      VB3(12, 14);
      k3b(0, 0);
      VB3(10, 11);
      k3b(1, SL3);
      VB(8);
      HB();
    }
    const int row0 = pm * 128 + kh * 64;
    const u32x4 s4 = mk_srd(w4, D * F * 2);
    const unsigned mb4 = 8192 + (lw & 1) * 4224;
    unsigned vb4[4];
#pragma unroll
    for (int i = 0; i < 4; ++i) vb4[i] = (112 * (lane >> 5) + 8 * (lw & 1) + 4 * (g & 1)) * 2048 + mm * 16 + j8 * 256 + i * 992;
    auto own4 = [&](int f) { dma4p(f * SL4 + mb4, vb4, s4, (pn * 224 + f * 16) * 2048); };
    {
      STAMP(7);
      gate(LV_X1, 2, lane < 16 && lane != pn ? flag_at(LV_X1, pm * 16 + mm) : WS_END, [&] {
        k3b(2, 2 * SL3);
        k3b(3, 3 * SL3);
      });
      STAMP(8);
#pragma unroll
      for (int h = 0; h < 4; ++h) k3a(h, h * SL3);
      VB(8);
      float csv;
      {
        const u32x4 ones = {0x3f803f80u, 0x3f803f80u, 0x3f803f80u, 0x3f803f80u};
        const unsigned a = 8192 + 7200 * (g & 1) + 448 * ((mm >> 2) + 8 * (g >> 1)) + 8 * (mm & 3) + lw * 128;
        f32x4 c[4];
#pragma unroll
        for (int f = 0; f < 4; ++f) {
          c[f] = (f32x4){0.f, 0.f, 0.f, 0.f};
#pragma unroll
          for (int h = 0; h < 2; ++h) {
            const u32x2 lo = tr_read(a + h * SL3 + f * 32), hi = tr_read(a + h * SL3 + f * 32 + 1792);
            c[f] = mfma16(ones, (u32x4){lo.x, lo.y, hi.x, hi.y}, c[f]);
          }
        }
        csv = g == 0 ? c[0][0] : g == 1 ? c[1][0] : g == 2 ? c[2][0] : c[3][0];
      }
      __builtin_amdgcn_raw_buffer_store_b64((u32x2){__float_as_uint(csv), tag}, rw, lw * 64 + lane < 224 ? lane * 8 : WS_END,
                                            WS_CS + (pn * 16 + pm) * 1792 + lw * 512, AUX_SC1);
      STAMP(9);
      unsigned m = 4 * SL3 + lw * 2048, ka = (((st3 + 4) & 15) | rd3) * 8192, kb = ka * 28, n = 24, c = (rd3 + 4) * 8192;
      asm volatile(
    "s_mov_b32 m0, %[m]\n"
    "s_waitcnt vmcnt(7)\n"
    "s_barrier\n"
    "buffer_load_dwordx4 %[a0], %[sa], %[ka] offen offset:0 sc1 lds\n"
    "buffer_load_dwordx4 %[a0], %[sa], %[ka] offen offset:1024 sc1 lds\n"
    "s_add_u32 m0, %[m], %[db]\n"
    "s_add_u32 %[c], %[c], 8192\n"
    "buffer_load_dwordx4 %[b0], %[sb], %[kb] offen offset:0 lds\n"
    "buffer_load_dwordx4 %[b1], %[sb], %[kb] offen offset:1024 lds\n"
    "buffer_load_dwordx4 %[b2], %[sb], %[kb] offen offset:2048 lds\n"
    "s_mov_b32 exec_hi, 0\n"
    "buffer_load_dwordx4 %[b3], %[sb], %[kb] offen offset:3072 lds\n"
    "s_mov_b32 exec_hi, -1\n"
    "s_add_u32 %[ka], %[c], %[s0]\n"
    "s_and_b32 %[ka], %[ka], 0x1ffff\n"
    "s_and_b32 %[kb], %[c], 0x20000\n"
    "s_or_b32 %[ka], %[ka], %[kb]\n"
    "s_mul_i32 %[kb], %[ka], 28\n"
    "s_add_u32 %[m], %[m], 22560\n"
    "s_cmp_lt_u32 %[m], 157920\n"
    "s_cselect_b32 %[m], %[m], %[mb]\n"
    "s_mov_b32 m0, %[m]\n"
    "s_waitcnt vmcnt(11)\n"
    "s_barrier\n"
    "buffer_load_dwordx4 %[a0], %[sa], %[ka] offen offset:0 sc1 lds\n"
    "buffer_load_dwordx4 %[a0], %[sa], %[ka] offen offset:1024 sc1 lds\n"
    "s_add_u32 m0, %[m], %[db]\n"
    "s_add_u32 %[c], %[c], 8192\n"
    "buffer_load_dwordx4 %[b0], %[sb], %[kb] offen offset:0 lds\n"
    "buffer_load_dwordx4 %[b1], %[sb], %[kb] offen offset:1024 lds\n"
    "buffer_load_dwordx4 %[b2], %[sb], %[kb] offen offset:2048 lds\n"
    "s_mov_b32 exec_hi, 0\n"
    "buffer_load_dwordx4 %[b3], %[sb], %[kb] offen offset:3072 lds\n"
    "s_mov_b32 exec_hi, -1\n"
    "s_add_u32 %[ka], %[c], %[s0]\n"
    "s_and_b32 %[ka], %[ka], 0x1ffff\n"
    "s_and_b32 %[kb], %[c], 0x20000\n"
    "s_or_b32 %[ka], %[ka], %[kb]\n"
    "s_mul_i32 %[kb], %[ka], 28\n"
    "s_add_u32 %[m], %[m], 22560\n"
    "s_cmp_lt_u32 %[m], 157920\n"
    "s_cselect_b32 %[m], %[m], %[mb]\n"
    "s_mov_b32 m0, %[m]\n"
    "s_waitcnt vmcnt(15)\n"
    "s_barrier\n"
    "buffer_load_dwordx4 %[a0], %[sa], %[ka] offen offset:0 sc1 lds\n"
    "buffer_load_dwordx4 %[a0], %[sa], %[ka] offen offset:1024 sc1 lds\n"
    "s_add_u32 m0, %[m], %[db]\n"
    "s_add_u32 %[c], %[c], 8192\n"
    "buffer_load_dwordx4 %[b0], %[sb], %[kb] offen offset:0 lds\n"
    "buffer_load_dwordx4 %[b1], %[sb], %[kb] offen offset:1024 lds\n"
    "buffer_load_dwordx4 %[b2], %[sb], %[kb] offen offset:2048 lds\n"
    "s_mov_b32 exec_hi, 0\n"
    "buffer_load_dwordx4 %[b3], %[sb], %[kb] offen offset:3072 lds\n"
    "s_mov_b32 exec_hi, -1\n"
    "s_add_u32 %[ka], %[c], %[s0]\n"
    "s_and_b32 %[ka], %[ka], 0x1ffff\n"
    "s_and_b32 %[kb], %[c], 0x20000\n"
    "s_or_b32 %[ka], %[ka], %[kb]\n"
    "s_mul_i32 %[kb], %[ka], 28\n"
    "s_add_u32 %[m], %[m], 22560\n"
    "s_cmp_lt_u32 %[m], 157920\n"
    "s_cselect_b32 %[m], %[m], %[mb]\n"
    "s_mov_b32 m0, %[m]\n"
    "1:\n"
    "s_waitcnt vmcnt(18)\n"
    "s_barrier\n"
    "buffer_load_dwordx4 %[a0], %[sa], %[ka] offen offset:0 sc1 lds\n"
    "buffer_load_dwordx4 %[a0], %[sa], %[ka] offen offset:1024 sc1 lds\n"
    "s_add_u32 m0, %[m], %[db]\n"
    "s_add_u32 %[c], %[c], 8192\n"
    "buffer_load_dwordx4 %[b0], %[sb], %[kb] offen offset:0 lds\n"
    "buffer_load_dwordx4 %[b1], %[sb], %[kb] offen offset:1024 lds\n"
    "buffer_load_dwordx4 %[b2], %[sb], %[kb] offen offset:2048 lds\n"
    "s_mov_b32 exec_hi, 0\n"
    "buffer_load_dwordx4 %[b3], %[sb], %[kb] offen offset:3072 lds\n"
    "s_mov_b32 exec_hi, -1\n"
    "s_add_u32 %[ka], %[c], %[s0]\n"
    "s_and_b32 %[ka], %[ka], 0x1ffff\n"
    "s_and_b32 %[kb], %[c], 0x20000\n"
    "s_or_b32 %[ka], %[ka], %[kb]\n"
    "s_mul_i32 %[kb], %[ka], 28\n"
    "s_add_u32 %[m], %[m], 22560\n"
    "s_cmp_lt_u32 %[m], 157920\n"
    "s_cselect_b32 %[m], %[m], %[mb]\n"
    "s_mov_b32 m0, %[m]\n"
    "s_sub_u32 %[n], %[n], 1\n"
    "s_cbranch_scc0 1b\n"
          : [m] "+&s"(m), [ka] "+&s"(ka), [kb] "+&s"(kb), [n] "+&s"(n), [c] "+&s"(c) HB_OUT
          : [s0] "s"(st3 * 8192), [a0] "v"(va3), [b0] "v"(vw3[0]), [b1] "v"(vw3[1]), [b2] "v"(vw3[2]), [b3] "v"(vw3[3]), [sa] "s"(ws), [sb] "s"(s3), [mb] "s"(lw * 2048),
            [db] "s"(b3 - lw * 2048) HB_IN
          : "memory", "scc", "vcc" HB_CLOB);
      const unsigned vy = flag_at(LV_Y, g ? pm * 16 + (pn ^ 8) : mm * 16 + 2 * j8 + (lw & 1));
      touch(lane < 3 ? ((lw * 3 + lane) * 32 + idx) * 128 : CODE_BYTES, mk_srd((const void*)pc, CODE_BYTES));
      unsigned py = ld1_async(vy, ws);
      u32x4 sp[4];
#pragma unroll
      for (int i = 0; i < 4; ++i) sp[i] = ld_async(g * 1024 + mm * 16, ws, WS_ST + (pm * 16 + i * 4) * 1024 + lw * 256);
      VB(24);
      u32x4 cp[8];
      const unsigned vc = (lane >> 5) * 1792 + min(lane & 31, 27) * 16;
#pragma unroll
      for (int i = 0; i < 8; ++i) cp[i] = ld_async(vc, ws, WS_CS + (pn * 16 + 2 * i) * 1792 + lw * 448);
      VB(26);
      if (lw >= 2) {
        own4(5);
        own4(6);
        VB(28);
        own4(0);
        own4(1);
        VB(24);
        wait_vm<16>();
      } else {
        VB(20);
        VB(8);
        wait_vm<0>();
      }
      asm volatile("" : "+v"(sp[0]), "+v"(sp[1]), "+v"(sp[2]), "+v"(sp[3])::"memory");
      {
        const f32x4 t = (__builtin_bit_cast(f32x4, sp[0]) + __builtin_bit_cast(f32x4, sp[1])) +
                        (__builtin_bit_cast(f32x4, sp[2]) + __builtin_bit_cast(f32x4, sp[3]));
        const float sm = red32(red16(t[0], t[1]), red16(t[2], t[3]));
        const float sq = __uint_as_float(__builtin_amdgcn_update_dpp(0u, __float_as_uint(sm), 0x128, 0xf, 0xf, false));
        const float mu = sm * (1.0f / D);
        const float rs = __builtin_amdgcn_rsqf(fmaxf(sq * (1.0f / D) - mu * mu, 0.f) + EPS);
        if (mm < 8) *L<f32x2>(L3_ROW + (lw * 32 + mm * 4 + g) * 8) = (f32x2){-mu, rs};
      }
      asm volatile("" : "+v"(py), "+v"(cp[0]), "+v"(cp[1]), "+v"(cp[2]), "+v"(cp[3]), "+v"(cp[4]), "+v"(cp[5]), "+v"(cp[6]), "+v"(cp[7])::"memory");
      float c0, c1;
      auto fold = [&](const u32x4(&w)[8]) {
        unsigned bad = 0;
        c0 = c1 = 0.f;
#pragma unroll
        for (int i = 0; i < 8; ++i) bad |= (w[i][1] ^ tag) | (w[i][3] ^ tag), c0 += __uint_as_float(w[i][0]), c1 += __uint_as_float(w[i][2]);
        return __builtin_amdgcn_ballot_w64(bad != 0) != 0;
      };
      if (fold(cp)) {
#pragma unroll 1
        for (int it = 0; it < SPIN; ++it) {
          u32x4 w[8];
          asm volatile("" ::: "memory");
#pragma unroll
          for (int i = 0; i < 8; ++i) w[i] = ld16<AUX_SC1>(rw, vc, WS_CS + (pn * 16 + 2 * i) * 1792 + lw * 448);
          if (!fold(w)) break;
        }
      }
      const float cc = red32(c0, c1);
      if ((lane & 31) < 28) *L<float>(L3_COL + (lw * 56 + (lane & 31) * 2 + (lane >> 5)) * 4) = cc;
      asm volatile("s_waitcnt lgkmcnt(0)" ::: "memory");
      HB();
      if (lw >= 2) {
        own4(2);
        own4(3);
      } else {
        if (!all_eq(py, tag + LV_Y)) spin1(vy, ws, tag + LV_Y);
#pragma unroll
        for (int i = 0; i < 8; ++i)
          dma1s(L_Y + (lw * 8 + i) * 1024, (lane >> 5) * 16384 + (lane & 31) * 16, ws, WS_Y + (j8 * 32 + lw * 16 + i * 2) * 16384 + row0 * 8);
      }
      HB();
    }
    {
      STAMP(10);
      unsigned t[8];
      auto tile = [&](int i) { return (j8 + 1 + (i + 2 * (pm & 3)) % 7) & 7; };
      if (lw >= 2) own4(4);
      touch((((kh * 4 + (xcd >> 1)) * 32 + idx) * 224 + lw * 56 + min(lane, 55)) * 128, s4);
      if (lw >= 2) {
#pragma unroll
        for (int i = 1; i < 8; ++i) t[i] = (kh * 8 + tile(i - 1)) * 224 * 2048;
        unsigned m = 7 * SL4 + mb4, n = 3;
        STAMP(11);
        VB(0);
        asm volatile(
    "s_mov_b32 m0, %[m]\n"
    "1:\n"
    "s_waitcnt vmcnt(16)\n"
    "s_barrier\n"
    "buffer_load_dwordx4 %[b0], %[sb], %[t1] offen offset:0 lds\n"
    "buffer_load_dwordx4 %[b1], %[sb], %[t1] offen offset:1056 lds\n"
    "buffer_load_dwordx4 %[b2], %[sb], %[t1] offen offset:2112 lds\n"
    "buffer_load_dwordx4 %[b3], %[sb], %[t1] offen offset:3168 lds\n"
    "s_add_u32 %[m], %[m], 16640\n"
    "s_cmp_lt_u32 %[m], 133120\n"
    "s_cselect_b32 %[m], %[m], %[mb]\n"
    "s_mov_b32 m0, %[m]\n"
    "s_nop 0\n"
    "buffer_load_dwordx4 %[b0], %[sb], %[t2] offen offset:0 lds\n"
    "buffer_load_dwordx4 %[b1], %[sb], %[t2] offen offset:1056 lds\n"
    "buffer_load_dwordx4 %[b2], %[sb], %[t2] offen offset:2112 lds\n"
    "buffer_load_dwordx4 %[b3], %[sb], %[t2] offen offset:3168 lds\n"
    "s_add_u32 %[m], %[m], 16640\n"
    "s_cmp_lt_u32 %[m], 133120\n"
    "s_cselect_b32 %[m], %[m], %[mb]\n"
    "s_mov_b32 m0, %[m]\n"
    "s_waitcnt vmcnt(16)\n"
    "s_barrier\n"
    "buffer_load_dwordx4 %[b0], %[sb], %[t3] offen offset:0 lds\n"
    "buffer_load_dwordx4 %[b1], %[sb], %[t3] offen offset:1056 lds\n"
    "buffer_load_dwordx4 %[b2], %[sb], %[t3] offen offset:2112 lds\n"
    "buffer_load_dwordx4 %[b3], %[sb], %[t3] offen offset:3168 lds\n"
    "s_add_u32 %[m], %[m], 16640\n"
    "s_cmp_lt_u32 %[m], 133120\n"
    "s_cselect_b32 %[m], %[m], %[mb]\n"
    "s_mov_b32 m0, %[m]\n"
    "s_nop 0\n"
    "buffer_load_dwordx4 %[b0], %[sb], %[t4] offen offset:0 lds\n"
    "buffer_load_dwordx4 %[b1], %[sb], %[t4] offen offset:1056 lds\n"
    "buffer_load_dwordx4 %[b2], %[sb], %[t4] offen offset:2112 lds\n"
    "buffer_load_dwordx4 %[b3], %[sb], %[t4] offen offset:3168 lds\n"
    "s_add_u32 %[m], %[m], 16640\n"
    "s_cmp_lt_u32 %[m], 133120\n"
    "s_cselect_b32 %[m], %[m], %[mb]\n"
    "s_mov_b32 m0, %[m]\n"
    "s_waitcnt vmcnt(16)\n"
    "s_barrier\n"
    "buffer_load_dwordx4 %[b0], %[sb], %[t5] offen offset:0 lds\n"
    "buffer_load_dwordx4 %[b1], %[sb], %[t5] offen offset:1056 lds\n"
    "buffer_load_dwordx4 %[b2], %[sb], %[t5] offen offset:2112 lds\n"
    "buffer_load_dwordx4 %[b3], %[sb], %[t5] offen offset:3168 lds\n"
    "s_add_u32 %[m], %[m], 16640\n"
    "s_cmp_lt_u32 %[m], 133120\n"
    "s_cselect_b32 %[m], %[m], %[mb]\n"
    "s_mov_b32 m0, %[m]\n"
    "s_nop 0\n"
    "buffer_load_dwordx4 %[b0], %[sb], %[t6] offen offset:0 lds\n"
    "buffer_load_dwordx4 %[b1], %[sb], %[t6] offen offset:1056 lds\n"
    "buffer_load_dwordx4 %[b2], %[sb], %[t6] offen offset:2112 lds\n"
    "buffer_load_dwordx4 %[b3], %[sb], %[t6] offen offset:3168 lds\n"
    "s_add_u32 %[m], %[m], 16640\n"
    "s_cmp_lt_u32 %[m], 133120\n"
    "s_cselect_b32 %[m], %[m], %[mb]\n"
    "s_mov_b32 m0, %[m]\n"
    "s_waitcnt vmcnt(16)\n"
    "s_barrier\n"
    "buffer_load_dwordx4 %[b0], %[sb], %[t7] offen offset:0 lds\n"
    "buffer_load_dwordx4 %[b1], %[sb], %[t7] offen offset:1056 lds\n"
    "buffer_load_dwordx4 %[b2], %[sb], %[t7] offen offset:2112 lds\n"
    "buffer_load_dwordx4 %[b3], %[sb], %[t7] offen offset:3168 lds\n"
    "s_add_u32 %[m], %[m], 16640\n"
    "s_cmp_lt_u32 %[m], 133120\n"
    "s_cselect_b32 %[m], %[m], %[mb]\n"
    "s_mov_b32 m0, %[m]\n"
    "v_add_u32 %[b0], 0x8000, %[b0]\n"
    "v_add_u32 %[b1], 0x8000, %[b1]\n"
    "v_add_u32 %[b2], 0x8000, %[b2]\n"
    "v_add_u32 %[b3], 0x8000, %[b3]\n"
    "s_sub_u32 %[n], %[n], 1\n"
    "s_cbranch_scc1 2f\n"
    "s_nop 0\n"
    "buffer_load_dwordx4 %[b0], %[sb], %[t1] offen offset:0 lds\n"
    "buffer_load_dwordx4 %[b1], %[sb], %[t1] offen offset:1056 lds\n"
    "buffer_load_dwordx4 %[b2], %[sb], %[t1] offen offset:2112 lds\n"
    "buffer_load_dwordx4 %[b3], %[sb], %[t1] offen offset:3168 lds\n"
    "s_add_u32 %[m], %[m], 16640\n"
    "s_cmp_lt_u32 %[m], 133120\n"
    "s_cselect_b32 %[m], %[m], %[mb]\n"
    "s_mov_b32 m0, %[m]\n"
    "s_waitcnt vmcnt(16)\n"
    "s_barrier\n"
    "buffer_load_dwordx4 %[b0], %[sb], %[t2] offen offset:0 lds\n"
    "buffer_load_dwordx4 %[b1], %[sb], %[t2] offen offset:1056 lds\n"
    "buffer_load_dwordx4 %[b2], %[sb], %[t2] offen offset:2112 lds\n"
    "buffer_load_dwordx4 %[b3], %[sb], %[t2] offen offset:3168 lds\n"
    "s_add_u32 %[m], %[m], 16640\n"
    "s_cmp_lt_u32 %[m], 133120\n"
    "s_cselect_b32 %[m], %[m], %[mb]\n"
    "s_mov_b32 m0, %[m]\n"
    "s_nop 0\n"
    "buffer_load_dwordx4 %[b0], %[sb], %[t3] offen offset:0 lds\n"
    "buffer_load_dwordx4 %[b1], %[sb], %[t3] offen offset:1056 lds\n"
    "buffer_load_dwordx4 %[b2], %[sb], %[t3] offen offset:2112 lds\n"
    "buffer_load_dwordx4 %[b3], %[sb], %[t3] offen offset:3168 lds\n"
    "s_add_u32 %[m], %[m], 16640\n"
    "s_cmp_lt_u32 %[m], 133120\n"
    "s_cselect_b32 %[m], %[m], %[mb]\n"
    "s_mov_b32 m0, %[m]\n"
    "s_waitcnt vmcnt(16)\n"
    "s_barrier\n"
    "buffer_load_dwordx4 %[b0], %[sb], %[t4] offen offset:0 lds\n"
    "buffer_load_dwordx4 %[b1], %[sb], %[t4] offen offset:1056 lds\n"
    "buffer_load_dwordx4 %[b2], %[sb], %[t4] offen offset:2112 lds\n"
    "buffer_load_dwordx4 %[b3], %[sb], %[t4] offen offset:3168 lds\n"
    "s_add_u32 %[m], %[m], 16640\n"
    "s_cmp_lt_u32 %[m], 133120\n"
    "s_cselect_b32 %[m], %[m], %[mb]\n"
    "s_mov_b32 m0, %[m]\n"
    "s_nop 0\n"
    "buffer_load_dwordx4 %[b0], %[sb], %[t5] offen offset:0 lds\n"
    "buffer_load_dwordx4 %[b1], %[sb], %[t5] offen offset:1056 lds\n"
    "buffer_load_dwordx4 %[b2], %[sb], %[t5] offen offset:2112 lds\n"
    "buffer_load_dwordx4 %[b3], %[sb], %[t5] offen offset:3168 lds\n"
    "s_add_u32 %[m], %[m], 16640\n"
    "s_cmp_lt_u32 %[m], 133120\n"
    "s_cselect_b32 %[m], %[m], %[mb]\n"
    "s_mov_b32 m0, %[m]\n"
    "s_waitcnt vmcnt(16)\n"
    "s_barrier\n"
    "buffer_load_dwordx4 %[b0], %[sb], %[t6] offen offset:0 lds\n"
    "buffer_load_dwordx4 %[b1], %[sb], %[t6] offen offset:1056 lds\n"
    "buffer_load_dwordx4 %[b2], %[sb], %[t6] offen offset:2112 lds\n"
    "buffer_load_dwordx4 %[b3], %[sb], %[t6] offen offset:3168 lds\n"
    "s_add_u32 %[m], %[m], 16640\n"
    "s_cmp_lt_u32 %[m], 133120\n"
    "s_cselect_b32 %[m], %[m], %[mb]\n"
    "s_mov_b32 m0, %[m]\n"
    "s_nop 0\n"
    "buffer_load_dwordx4 %[b0], %[sb], %[t7] offen offset:0 lds\n"
    "buffer_load_dwordx4 %[b1], %[sb], %[t7] offen offset:1056 lds\n"
    "buffer_load_dwordx4 %[b2], %[sb], %[t7] offen offset:2112 lds\n"
    "buffer_load_dwordx4 %[b3], %[sb], %[t7] offen offset:3168 lds\n"
    "s_add_u32 %[m], %[m], 16640\n"
    "s_cmp_lt_u32 %[m], 133120\n"
    "s_cselect_b32 %[m], %[m], %[mb]\n"
    "s_mov_b32 m0, %[m]\n"
    "v_add_u32 %[b0], 0x8000, %[b0]\n"
    "v_add_u32 %[b1], 0x8000, %[b1]\n"
    "v_add_u32 %[b2], 0x8000, %[b2]\n"
    "v_add_u32 %[b3], 0x8000, %[b3]\n"
    "s_branch 1b\n"
    "2:\n"
            : [m] "+&s"(m), [n] "+&s"(n), [b0] "+&v"(vb4[0]), [b1] "+&v"(vb4[1]), [b2] "+&v"(vb4[2]), [b3] "+&v"(vb4[3]) HB_OUT
            : [sb] "s"(s4), [mb] "s"(mb4), [t1] "s"(t[1]), [t2] "s"(t[2]), [t3] "s"(t[3]), [t4] "s"(t[4]), [t5] "s"(t[5]), [t6] "s"(t[6]),
              [t7] "s"(t[7]) HB_IN
            : "memory", "scc", "vcc" HB_CLOB);
      } else {
        const unsigned vl = flag_at(LV_L0, pm * 16 + kh * 8 + ((j8 + 1 + lane) & 7)), vs = L_LSCR + min(lane, 6) * 4;
        unsigned va = WS_H3 + (pm * 16 + kh * 8) * 57344 + lw * 4096 + l16, m = 7 * SL4 + lw * 4096, n, k, vt, wt, sp, lo;
#pragma unroll
        for (int i = 1; i < 8; ++i) t[i] = tile(i - 1) * 57344;
        if (lw == 0) {
          spin_lds(L_CNT + 8, 12);
          if (lane == 0) __builtin_amdgcn_raw_buffer_store_b32(tag + LV_L0, rw, 0, flag_at(LV_L0, pm * 16 + pn), AUX_SC1);
        } else {
#pragma unroll 1
          for (int i = 0; i < SPIN; ++i) {
            __builtin_amdgcn_s_sleep(6);
            if (ready_scr(vs, tag + LV_L0) || lds_word(L_CNT + 4)) break;
            if (i >= 2) dma_poll(L_LSCR, vl, ws, 0);
          }
        }
        STAMP(11);
        HB();
        asm volatile(
    "s_mov_b32 %[n], 0\n"
    "s_mov_b32 %[k], 0\n"
    "s_mov_b32 %[lo], 0\n"
    "s_mov_b32 %[wt], %[w0]\n"
    "4:\n"
    "ds_read_b32 %[vt], %[vs]\n"
    "s_waitcnt lgkmcnt(0)\n"
    "v_cmp_ne_u32 vcc, %[wt], %[vt]\n"
    "s_cbranch_vccz 3f\n"
    "s_mov_b32 m0, %[scr]\n"
    "s_mov_b64 exec, %[em]\n"
    "buffer_load_dword %[vl], %[sa], %[lo] offen sc1 lds\n"
    "s_mov_b64 exec, -1\n"
    "s_waitcnt vmcnt(63)\n"
    "s_barrier\n"
    "s_add_u32 %[k], %[k], 2\n"
    "s_cmp_lt_u32 %[k], 6\n"
    "s_cbranch_scc1 4b\n"
    "ds_read_b32 %[vt], %[vs]\n"
    "s_waitcnt lgkmcnt(0)\n"
    "v_cmp_ne_u32 vcc, %[wt], %[vt]\n"
    "s_cbranch_vccz 3f\n"
    "s_mov_b32 %[sp], 0x10000\n"
    "2:\n"
    "s_mov_b32 m0, %[scr]\n"
    "s_mov_b64 exec, 0x7f\n"
    "buffer_load_dword %[vl], %[sa], %[lo] offen sc1 lds\n"
    "s_mov_b64 exec, -1\n"
    "s_waitcnt vmcnt(0)\n"
    "ds_read_b32 %[vt], %[vs]\n"
    "s_waitcnt lgkmcnt(0)\n"
    "v_cmp_ne_u32 vcc, %[wt], %[vt]\n"
    "s_cbranch_vccz 3f\n"
    "s_sub_u32 %[sp], %[sp], 1\n"
    "s_cbranch_scc0 2b\n"
    "3:\n"
    "s_mov_b32 m0, %[m]\n"
    "1:\n"
    "s_cmp_gt_u32 %[k], 0\n"
    "s_cbranch_scc1 5f\n"
    "s_waitcnt vmcnt(16)\n"
    "s_barrier\n"
    "5:\n"
    "buffer_load_dwordx4 %[a0], %[sa], %[t1] offen offset:0 sc1 lds\n"
    "buffer_load_dwordx4 %[a0], %[sa], %[t1] offen offset:1024 sc1 lds\n"
    "buffer_load_dwordx4 %[a0], %[sa], %[t1] offen offset:2048 sc1 lds\n"
    "buffer_load_dwordx4 %[a0], %[sa], %[t1] offen offset:3072 sc1 lds\n"
    "s_add_u32 %[m], %[m], 16640\n"
    "s_cmp_lt_u32 %[m], 133120\n"
    "s_cselect_b32 %[m], %[m], %[mb]\n"
    "s_add_u32 %[n], %[n], 1\n"
    "s_lshr_b32 %[wt], %[n], 1\n"
    "s_min_u32 %[wt], %[wt], 2\n"
    "s_mov_b32 m0, %[m]\n"
    "s_nop 0\n"
    "buffer_load_dwordx4 %[a0], %[sa], %[t2] offen offset:0 sc1 lds\n"
    "buffer_load_dwordx4 %[a0], %[sa], %[t2] offen offset:1024 sc1 lds\n"
    "buffer_load_dwordx4 %[a0], %[sa], %[t2] offen offset:2048 sc1 lds\n"
    "buffer_load_dwordx4 %[a0], %[sa], %[t2] offen offset:3072 sc1 lds\n"
    "s_add_u32 %[m], %[m], 16640\n"
    "s_cmp_lt_u32 %[m], 133120\n"
    "s_cselect_b32 %[m], %[m], %[mb]\n"
    "s_lshl_b32 %[lo], %[wt], 15\n"
    "s_add_u32 %[wt], %[wt], %[w0]\n"
    "s_mov_b32 m0, %[m]\n"
    "s_cmp_gt_u32 %[k], 2\n"
    "s_cbranch_scc1 5f\n"
    "s_waitcnt vmcnt(16)\n"
    "s_barrier\n"
    "5:\n"
    "buffer_load_dwordx4 %[a0], %[sa], %[t3] offen offset:0 sc1 lds\n"
    "buffer_load_dwordx4 %[a0], %[sa], %[t3] offen offset:1024 sc1 lds\n"
    "buffer_load_dwordx4 %[a0], %[sa], %[t3] offen offset:2048 sc1 lds\n"
    "buffer_load_dwordx4 %[a0], %[sa], %[t3] offen offset:3072 sc1 lds\n"
    "s_add_u32 %[m], %[m], 16640\n"
    "s_cmp_lt_u32 %[m], 133120\n"
    "s_cselect_b32 %[m], %[m], %[mb]\n"
    "s_mov_b32 m0, %[scr]\n"
    "s_mov_b64 exec, %[em]\n"
    "buffer_load_dword %[vl], %[sa], %[lo] offen sc1 lds\n"
    "s_mov_b64 exec, -1\n"
    "s_mov_b32 m0, %[m]\n"
    "s_nop 0\n"
    "buffer_load_dwordx4 %[a0], %[sa], %[t4] offen offset:0 sc1 lds\n"
    "buffer_load_dwordx4 %[a0], %[sa], %[t4] offen offset:1024 sc1 lds\n"
    "buffer_load_dwordx4 %[a0], %[sa], %[t4] offen offset:2048 sc1 lds\n"
    "buffer_load_dwordx4 %[a0], %[sa], %[t4] offen offset:3072 sc1 lds\n"
    "s_add_u32 %[m], %[m], 16640\n"
    "s_cmp_lt_u32 %[m], 133120\n"
    "s_cselect_b32 %[m], %[m], %[mb]\n"
    "s_mov_b32 m0, %[m]\n"
    "s_cmp_gt_u32 %[k], 4\n"
    "s_cbranch_scc1 5f\n"
    "s_waitcnt vmcnt(16)\n"
    "s_barrier\n"
    "5:\n"
    "buffer_load_dwordx4 %[a0], %[sa], %[t5] offen offset:0 sc1 lds\n"
    "buffer_load_dwordx4 %[a0], %[sa], %[t5] offen offset:1024 sc1 lds\n"
    "buffer_load_dwordx4 %[a0], %[sa], %[t5] offen offset:2048 sc1 lds\n"
    "buffer_load_dwordx4 %[a0], %[sa], %[t5] offen offset:3072 sc1 lds\n"
    "s_add_u32 %[m], %[m], 16640\n"
    "s_cmp_lt_u32 %[m], 133120\n"
    "s_cselect_b32 %[m], %[m], %[mb]\n"
    "s_mov_b32 m0, %[scr]\n"
    "s_mov_b64 exec, %[em]\n"
    "buffer_load_dword %[vl], %[sa], %[lo] offen sc1 lds\n"
    "s_mov_b64 exec, -1\n"
    "s_mov_b32 m0, %[m]\n"
    "s_nop 0\n"
    "buffer_load_dwordx4 %[a0], %[sa], %[t6] offen offset:0 sc1 lds\n"
    "buffer_load_dwordx4 %[a0], %[sa], %[t6] offen offset:1024 sc1 lds\n"
    "buffer_load_dwordx4 %[a0], %[sa], %[t6] offen offset:2048 sc1 lds\n"
    "buffer_load_dwordx4 %[a0], %[sa], %[t6] offen offset:3072 sc1 lds\n"
    "s_add_u32 %[m], %[m], 16640\n"
    "s_cmp_lt_u32 %[m], 133120\n"
    "s_cselect_b32 %[m], %[m], %[mb]\n"
    "s_mov_b32 m0, %[m]\n"
    "s_waitcnt vmcnt(16)\n"
    "s_barrier\n"
    "ds_read_b32 %[vt], %[vs]\n"
    "buffer_load_dwordx4 %[a0], %[sa], %[t7] offen offset:0 sc1 lds\n"
    "buffer_load_dwordx4 %[a0], %[sa], %[t7] offen offset:1024 sc1 lds\n"
    "buffer_load_dwordx4 %[a0], %[sa], %[t7] offen offset:2048 sc1 lds\n"
    "buffer_load_dwordx4 %[a0], %[sa], %[t7] offen offset:3072 sc1 lds\n"
    "s_add_u32 %[m], %[m], 16640\n"
    "s_cmp_lt_u32 %[m], 133120\n"
    "s_cselect_b32 %[m], %[m], %[mb]\n"
    "s_mov_b32 %[k], 0\n"
    "s_cmp_eq_u32 %[n], 7\n"
    "s_cbranch_scc1 6f\n"
    "v_add_u32 %[a0], 0x2000, %[a0]\n"
    "s_waitcnt lgkmcnt(0)\n"
    "v_cmp_ne_u32 vcc, %[wt], %[vt]\n"
    "s_cbranch_vccz 3f\n"
    "s_mov_b32 %[sp], 0x10000\n"
    "2:\n"
    "s_mov_b32 m0, %[scr]\n"
    "s_mov_b64 exec, 0x7f\n"
    "buffer_load_dword %[vl], %[sa], %[lo] offen sc1 lds\n"
    "s_mov_b64 exec, -1\n"
    "s_waitcnt vmcnt(0)\n"
    "ds_read_b32 %[vt], %[vs]\n"
    "s_waitcnt lgkmcnt(0)\n"
    "v_cmp_ne_u32 vcc, %[wt], %[vt]\n"
    "s_cbranch_vccz 3f\n"
    "s_sub_u32 %[sp], %[sp], 1\n"
    "s_cbranch_scc0 2b\n"
    "3:\n"
    "s_mov_b32 m0, %[m]\n"
    "s_nop 0\n"
    "buffer_load_dwordx4 %[a0], %[sa], %[t1] offen offset:0 sc1 lds\n"
    "buffer_load_dwordx4 %[a0], %[sa], %[t1] offen offset:1024 sc1 lds\n"
    "buffer_load_dwordx4 %[a0], %[sa], %[t1] offen offset:2048 sc1 lds\n"
    "buffer_load_dwordx4 %[a0], %[sa], %[t1] offen offset:3072 sc1 lds\n"
    "s_add_u32 %[m], %[m], 16640\n"
    "s_cmp_lt_u32 %[m], 133120\n"
    "s_cselect_b32 %[m], %[m], %[mb]\n"
    "s_add_u32 %[n], %[n], 1\n"
    "s_lshr_b32 %[wt], %[n], 1\n"
    "s_min_u32 %[wt], %[wt], 2\n"
    "s_mov_b32 m0, %[m]\n"
    "s_waitcnt vmcnt(16)\n"
    "s_barrier\n"
    "buffer_load_dwordx4 %[a0], %[sa], %[t2] offen offset:0 sc1 lds\n"
    "buffer_load_dwordx4 %[a0], %[sa], %[t2] offen offset:1024 sc1 lds\n"
    "buffer_load_dwordx4 %[a0], %[sa], %[t2] offen offset:2048 sc1 lds\n"
    "buffer_load_dwordx4 %[a0], %[sa], %[t2] offen offset:3072 sc1 lds\n"
    "s_add_u32 %[m], %[m], 16640\n"
    "s_cmp_lt_u32 %[m], 133120\n"
    "s_cselect_b32 %[m], %[m], %[mb]\n"
    "s_lshl_b32 %[lo], %[wt], 15\n"
    "s_add_u32 %[wt], %[wt], %[w0]\n"
    "s_mov_b32 m0, %[m]\n"
    "s_nop 0\n"
    "buffer_load_dwordx4 %[a0], %[sa], %[t3] offen offset:0 sc1 lds\n"
    "buffer_load_dwordx4 %[a0], %[sa], %[t3] offen offset:1024 sc1 lds\n"
    "buffer_load_dwordx4 %[a0], %[sa], %[t3] offen offset:2048 sc1 lds\n"
    "buffer_load_dwordx4 %[a0], %[sa], %[t3] offen offset:3072 sc1 lds\n"
    "s_add_u32 %[m], %[m], 16640\n"
    "s_cmp_lt_u32 %[m], 133120\n"
    "s_cselect_b32 %[m], %[m], %[mb]\n"
    "s_mov_b32 m0, %[scr]\n"
    "s_mov_b64 exec, %[em]\n"
    "buffer_load_dword %[vl], %[sa], %[lo] offen sc1 lds\n"
    "s_mov_b64 exec, -1\n"
    "s_mov_b32 m0, %[m]\n"
    "s_waitcnt vmcnt(16)\n"
    "s_barrier\n"
    "buffer_load_dwordx4 %[a0], %[sa], %[t4] offen offset:0 sc1 lds\n"
    "buffer_load_dwordx4 %[a0], %[sa], %[t4] offen offset:1024 sc1 lds\n"
    "buffer_load_dwordx4 %[a0], %[sa], %[t4] offen offset:2048 sc1 lds\n"
    "buffer_load_dwordx4 %[a0], %[sa], %[t4] offen offset:3072 sc1 lds\n"
    "s_add_u32 %[m], %[m], 16640\n"
    "s_cmp_lt_u32 %[m], 133120\n"
    "s_cselect_b32 %[m], %[m], %[mb]\n"
    "s_mov_b32 m0, %[m]\n"
    "s_nop 0\n"
    "buffer_load_dwordx4 %[a0], %[sa], %[t5] offen offset:0 sc1 lds\n"
    "buffer_load_dwordx4 %[a0], %[sa], %[t5] offen offset:1024 sc1 lds\n"
    "buffer_load_dwordx4 %[a0], %[sa], %[t5] offen offset:2048 sc1 lds\n"
    "buffer_load_dwordx4 %[a0], %[sa], %[t5] offen offset:3072 sc1 lds\n"
    "s_add_u32 %[m], %[m], 16640\n"
    "s_cmp_lt_u32 %[m], 133120\n"
    "s_cselect_b32 %[m], %[m], %[mb]\n"
    "s_mov_b32 m0, %[scr]\n"
    "s_mov_b64 exec, %[em]\n"
    "buffer_load_dword %[vl], %[sa], %[lo] offen sc1 lds\n"
    "s_mov_b64 exec, -1\n"
    "s_mov_b32 m0, %[m]\n"
    "s_waitcnt vmcnt(16)\n"
    "s_barrier\n"
    "buffer_load_dwordx4 %[a0], %[sa], %[t6] offen offset:0 sc1 lds\n"
    "buffer_load_dwordx4 %[a0], %[sa], %[t6] offen offset:1024 sc1 lds\n"
    "buffer_load_dwordx4 %[a0], %[sa], %[t6] offen offset:2048 sc1 lds\n"
    "buffer_load_dwordx4 %[a0], %[sa], %[t6] offen offset:3072 sc1 lds\n"
    "s_add_u32 %[m], %[m], 16640\n"
    "s_cmp_lt_u32 %[m], 133120\n"
    "s_cselect_b32 %[m], %[m], %[mb]\n"
    "s_mov_b32 m0, %[m]\n"
    "s_nop 0\n"
    "ds_read_b32 %[vt], %[vs]\n"
    "buffer_load_dwordx4 %[a0], %[sa], %[t7] offen offset:0 sc1 lds\n"
    "buffer_load_dwordx4 %[a0], %[sa], %[t7] offen offset:1024 sc1 lds\n"
    "buffer_load_dwordx4 %[a0], %[sa], %[t7] offen offset:2048 sc1 lds\n"
    "buffer_load_dwordx4 %[a0], %[sa], %[t7] offen offset:3072 sc1 lds\n"
    "s_add_u32 %[m], %[m], 16640\n"
    "s_cmp_lt_u32 %[m], 133120\n"
    "s_cselect_b32 %[m], %[m], %[mb]\n"
    "v_add_u32 %[a0], 0x2000, %[a0]\n"
    "s_waitcnt lgkmcnt(0)\n"
    "v_cmp_ne_u32 vcc, %[wt], %[vt]\n"
    "s_cbranch_vccz 3f\n"
    "s_mov_b32 %[sp], 0x10000\n"
    "2:\n"
    "s_mov_b32 m0, %[scr]\n"
    "s_mov_b64 exec, 0x7f\n"
    "buffer_load_dword %[vl], %[sa], %[lo] offen sc1 lds\n"
    "s_mov_b64 exec, -1\n"
    "s_waitcnt vmcnt(0)\n"
    "ds_read_b32 %[vt], %[vs]\n"
    "s_waitcnt lgkmcnt(0)\n"
    "v_cmp_ne_u32 vcc, %[wt], %[vt]\n"
    "s_cbranch_vccz 3f\n"
    "s_sub_u32 %[sp], %[sp], 1\n"
    "s_cbranch_scc0 2b\n"
    "3:\n"
    "s_mov_b32 m0, %[m]\n"
    "s_branch 1b\n"
    "6:\n"
    "s_waitcnt lgkmcnt(0)\n"
            : [m] "+&s"(m), [n] "=&s"(n), [k] "=&s"(k), [a0] "+&v"(va), [vt] "=&v"(vt), [wt] "=&s"(wt), [sp] "=&s"(sp), [lo] "=&s"(lo) HB_OUT
            : [sa] "s"(ws), [mb] "s"(lw * 4096), [t1] "s"(t[1]), [t2] "s"(t[2]), [t3] "s"(t[3]), [t4] "s"(t[4]), [t5] "s"(t[5]), [t6] "s"(t[6]),
              [t7] "s"(t[7]), [vl] "v"(vl), [vs] "v"(vs), [scr] "s"(L_LSCR), [em] "s"(lw ? 0x70ull : 0xfull),
              [w0] "s"(__builtin_amdgcn_readfirstlane(tag + LV_L0)) HB_IN
            : "memory", "scc", "vcc" HB_CLOB);
      }
      STAMP(12);
#pragma unroll
      for (int i = 0; i < 4; ++i) dma1((lw + 4 * i) * 1024, (4 * lw + g) * 2048 + ((mm ^ (4 * lw + g)) << 4), sx, (row0 + 16 * i) * 2048 + j8 * 256);
      VB(16);
#pragma unroll
      for (int i = 0; i < 4; ++i) dma1s(SL4 + (lw * 4 + i) * 1024, l16, ws, WS_X1 + (pm * 128 + j8 * 16 + lw * 4 + i) * 2048 + kh * 1024);
      VB(12);
      VB(8);
      VB(0);
      if (blockIdx.x == 0 && lw == 0 && lane == 0) __builtin_amdgcn_raw_buffer_store_b32(tag, rw, 0, WS_EP, AUX_SC1);
    }
    PROF_END();
    HB_END();
    return;
  }

  const unsigned epv = __builtin_amdgcn_raw_buffer_load_b32(rw, 0, WS_EP, AUX_SC1);
  const unsigned fo = flag_at(0, pm * 16 + pn);
  const unsigned cvb = CV + wave * CVW;
  LANES();
  const unsigned dbits = ssd[pn * 64 + pm * 4 + wave];
  const int un = pn ^ 1 ^ (pm & 1) << 3;
  {
    const int tq = lane >> 3, dq = lane & 7, t0 = pm * 128 + wave * 32 + tq * 4, d0 = un * 64 + dq * 8;
    const rsrc_t rk = mk_rsrc(kk, S * D * 2), rv = mk_rsrc(vv, S * D * 2);
    u32x4 kr[7], vr[4], sk[4];
#pragma unroll
    for (int j = 0; j < 4; ++j) sk[j] = *(const u32x4*)(shk + d0 * 4 + j * 8);
#pragma unroll
    for (int j = 0; j < 7; ++j) kr[j] = ld16<AUX_NT>(rk, (max(t0 - 3 + j, 0) * D + d0) * 2, 0);
#pragma unroll
    for (int j = 0; j < 4; ++j) vr[j] = ld16<AUX_NT>(rv, ((t0 + j) * D + d0) * 2, 0);
    FENCE();
    if (t0 == 0) kr[0] = kr[1] = kr[2] = (u32x4){0u, 0u, 0u, 0u};
    const unsigned ub = CV + (dq >> 1) * CVW + (dq & 1) * 2048 + (((15 - 4 * wave - (tq >> 1)) ^ dq) << 4) + ((~tq & 1) << 3);
#pragma unroll
    for (int c = 0; c < 4; ++c) {
      const f32x2 s0 = {bf_lo(sk[c][0]), bf_lo(sk[c][2])}, s1 = {bf_hi(sk[c][0]), bf_hi(sk[c][2])};
      const f32x2 s2 = {bf_lo(sk[c][1]), bf_lo(sk[c][3])}, s3 = {bf_hi(sk[c][1]), bf_hi(sk[c][3])};
      f32x2 kf[7], u[4];
#pragma unroll
      for (int j = 0; j < 7; ++j) kf[j] = bf_pair(kr[j][c]);
#pragma unroll
      for (int j = 0; j < 4; ++j) u[j] = fma2(kf[j + 3], s0, fma2(kf[j + 2], s1, fma2(kf[j + 1], s2, kf[j] * s3))) * bf_pair(vr[j][c]);
      *L<u32x2>(ub + (2 * c) * 256) = (u32x2){pack_bf16(u[3].x, u[2].x), pack_bf16(u[1].x, u[0].x)};
      *L<u32x2>(ub + (2 * c + 1) * 256) = (u32x2){pack_bf16(u[3].y, u[2].y), pack_bf16(u[1].y, u[0].y)};
    }
  }
  const unsigned tagv = __builtin_amdgcn_readfirstlane(epv) + 8;
  STAMP(1);
  LBAR();
  STAMP(2);
#pragma unroll
  for (int i = 0; i < 4; ++i)
    st16<AUX_SC1>(rw, (i * 4 + g) * 4096 + mm * 16, WS_U + (un * 64 + wave * 16) * 4096 + (S - 128 - pm * 128) * 2,
                  *L<u32x4>(cvb + (i * 4 + g) * 256 + ((mm ^ (2 * wave + (i >> 1))) << 4)));
#define VB64 (SL1_B + 1056 * q + 128 * (g & 1) + 512 * (g >> 1) + 8 * p)
  f32x32 acc2;
  {
    f32x32 acc;
    f32x4 rsum[2], rsq[2], csum;
    const unsigned vb64 = VB64;
    unsigned va = 1056 * mm + 256 * wave + 16 * g, vb0 = vb64 + 32 * wave, vb1 = vb64 + 32 * (1 ^ wave), vb2 = vb64 + 32 * (2 ^ wave),
             vb3 = vb64 + 32 * (3 ^ wave), sf, sc;
    asm volatile(
    "ds_read_b128 v[64:67], %[va] offset:0\n"
    "ds_read_b128 v[68:71], %[va] offset:128\n"
    "ds_read_b64_tr_b16 v[72:73], %[vb0] offset:0\n"
    "ds_read_b64_tr_b16 v[74:75], %[vb0] offset:256\n"
    "ds_read_b64_tr_b16 v[76:77], %[vb1] offset:0\n"
    "ds_read_b64_tr_b16 v[78:79], %[vb1] offset:256\n"
    "ds_read_b64_tr_b16 v[80:81], %[vb2] offset:0\n"
    "ds_read_b64_tr_b16 v[82:83], %[vb2] offset:256\n"
    "ds_read_b64_tr_b16 v[84:85], %[vb3] offset:0\n"
    "ds_read_b64_tr_b16 v[86:87], %[vb3] offset:256\n"
    "v_mov_b64 v[128:129], 0\n"
    "v_mov_b64 v[130:131], 0\n"
    "v_mov_b64 v[132:133], 0\n"
    "v_mov_b64 v[134:135], 0\n"
    "v_mov_b64 v[136:137], 0\n"
    "v_mov_b64 v[138:139], 0\n"
    "v_mov_b64 v[140:141], 0\n"
    "v_mov_b64 v[142:143], 0\n"
    "v_mov_b64 v[144:145], 0\n"
    "v_mov_b64 v[146:147], 0\n"
    "v_mov_b64 v[148:149], 0\n"
    "v_mov_b64 v[150:151], 0\n"
    "v_mov_b64 v[152:153], 0\n"
    "v_mov_b64 v[154:155], 0\n"
    "v_mov_b64 v[156:157], 0\n"
    "v_mov_b64 v[158:159], 0\n"
    "v_mov_b64 v[160:161], 0\n"
    "v_mov_b64 v[162:163], 0\n"
    "v_mov_b64 v[164:165], 0\n"
    "v_mov_b64 v[166:167], 0\n"
    "v_mov_b64 v[168:169], 0\n"
    "v_mov_b64 v[170:171], 0\n"
    "v_mov_b64 v[172:173], 0\n"
    "v_mov_b64 v[174:175], 0\n"
    "v_mov_b64 v[176:177], 0\n"
    "v_mov_b64 v[178:179], 0\n"
    "v_mov_b32 v112, 0x3f803f80\n"
    "v_mov_b32 v113, 0x3f803f80\n"
    "v_mov_b32 v114, 0x3f803f80\n"
    "v_mov_b32 v115, 0x3f803f80\n"
    "s_mov_b32 %[sf], 51712\n"
    "s_mov_b32 %[sc], 7\n"
    "s_waitcnt lgkmcnt(0)\n"
    "1:\n"
    "v_mfma_f32_16x16x32_bf16 v[128:131], v[72:75], v[64:67], v[128:131]\n"
    "ds_read_b128 v[88:91], %[va] offset:64\n"
    "ds_read_b128 v[92:95], %[va] offset:192\n"
    "v_mfma_f32_16x16x32_bf16 v[144:147], v[72:75], v[68:71], v[144:147]\n"
    "ds_read_b64_tr_b16 v[96:97], %[vb0] offset:4224\n"
    "ds_read_b64_tr_b16 v[98:99], %[vb0] offset:4480\n"
    "v_mfma_f32_16x16x32_bf16 v[132:135], v[76:79], v[64:67], v[132:135]\n"
    "ds_read_b64_tr_b16 v[100:101], %[vb1] offset:4224\n"
    "ds_read_b64_tr_b16 v[102:103], %[vb1] offset:4480\n"
    "v_mfma_f32_16x16x32_bf16 v[148:151], v[76:79], v[68:71], v[148:151]\n"
    "ds_read_b64_tr_b16 v[104:105], %[vb2] offset:4224\n"
    "ds_read_b64_tr_b16 v[106:107], %[vb2] offset:4480\n"
    "v_mfma_f32_16x16x32_bf16 v[136:139], v[80:83], v[64:67], v[136:139]\n"
    "ds_read_b64_tr_b16 v[108:109], %[vb3] offset:4224\n"
    "ds_read_b64_tr_b16 v[110:111], %[vb3] offset:4480\n"
    "v_mfma_f32_16x16x32_bf16 v[152:155], v[80:83], v[68:71], v[152:155]\n"
    "s_cmp_lg_u32 %[sc], 4\n"
    "v_mfma_f32_16x16x32_bf16 v[140:143], v[84:87], v[64:67], v[140:143]\n"
    "v_mfma_f32_16x16x32_bf16 v[156:159], v[84:87], v[68:71], v[156:159]\n"
    "s_cbranch_scc1 2f\n"
    "s_waitcnt vmcnt(0)\n"
    "2:\n"
    "v_mfma_f32_16x16x32_bf16 v[160:163], v[112:115], v[64:67], v[160:163]\n"
    "v_mfma_f32_16x16x32_bf16 v[164:167], v[112:115], v[68:71], v[164:167]\n"
    "v_mfma_f32_16x16x32_bf16 v[168:171], v[64:67], v[64:67], v[168:171]\n"
    "v_mfma_f32_16x16x32_bf16 v[172:175], v[68:71], v[68:71], v[172:175]\n"
    "v_mfma_f32_16x16x32_bf16 v[176:179], v[72:75], v[112:115], v[176:179]\n"
    "s_waitcnt lgkmcnt(0)\n"
    "s_barrier\n"
    "v_mfma_f32_16x16x32_bf16 v[128:131], v[96:99], v[88:91], v[128:131]\n"
    "ds_read_b128 v[64:67], %[va] offset:25856\n"
    "ds_read_b128 v[68:71], %[va] offset:25984\n"
    "v_mfma_f32_16x16x32_bf16 v[144:147], v[96:99], v[92:95], v[144:147]\n"
    "ds_read_b64_tr_b16 v[72:73], %[vb0] offset:25856\n"
    "ds_read_b64_tr_b16 v[74:75], %[vb0] offset:26112\n"
    "v_mfma_f32_16x16x32_bf16 v[132:135], v[100:103], v[88:91], v[132:135]\n"
    "ds_read_b64_tr_b16 v[76:77], %[vb1] offset:25856\n"
    "ds_read_b64_tr_b16 v[78:79], %[vb1] offset:26112\n"
    "v_mfma_f32_16x16x32_bf16 v[148:151], v[100:103], v[92:95], v[148:151]\n"
    "ds_read_b64_tr_b16 v[80:81], %[vb2] offset:25856\n"
    "ds_read_b64_tr_b16 v[82:83], %[vb2] offset:26112\n"
    "v_mfma_f32_16x16x32_bf16 v[136:139], v[104:107], v[88:91], v[136:139]\n"
    "ds_read_b64_tr_b16 v[84:85], %[vb3] offset:25856\n"
    "ds_read_b64_tr_b16 v[86:87], %[vb3] offset:26112\n"
    "v_mfma_f32_16x16x32_bf16 v[152:155], v[104:107], v[92:95], v[152:155]\n"
    "s_cmp_lg_u32 %[sc], %[hk]\n"
    "v_mfma_f32_16x16x32_bf16 v[140:143], v[108:111], v[88:91], v[140:143]\n"
    "v_mfma_f32_16x16x32_bf16 v[156:159], v[108:111], v[92:95], v[156:159]\n"
    "s_cbranch_scc1 3f\n"
    "s_mov_b64 exec, 1\n"
    "buffer_store_dword %[ft], off, %[ws], %[fo] sc1\n"
    "s_mov_b64 exec, -1\n"
    "3:\n"
    "v_mfma_f32_16x16x32_bf16 v[160:163], v[112:115], v[88:91], v[160:163]\n"
    "v_mfma_f32_16x16x32_bf16 v[164:167], v[112:115], v[92:95], v[164:167]\n"
    "v_mfma_f32_16x16x32_bf16 v[168:171], v[88:91], v[88:91], v[168:171]\n"
    "v_mfma_f32_16x16x32_bf16 v[172:175], v[92:95], v[92:95], v[172:175]\n"
    "v_mfma_f32_16x16x32_bf16 v[176:179], v[96:99], v[112:115], v[176:179]\n"
    "s_waitcnt lgkmcnt(0)\n"
    "v_mfma_f32_16x16x32_bf16 v[128:131], v[72:75], v[64:67], v[128:131]\n"
    "ds_read_b128 v[88:91], %[va] offset:25920\n"
    "ds_read_b128 v[92:95], %[va] offset:26048\n"
    "v_mfma_f32_16x16x32_bf16 v[144:147], v[72:75], v[68:71], v[144:147]\n"
    "ds_read_b64_tr_b16 v[96:97], %[vb0] offset:30080\n"
    "ds_read_b64_tr_b16 v[98:99], %[vb0] offset:30336\n"
    "v_mfma_f32_16x16x32_bf16 v[132:135], v[76:79], v[64:67], v[132:135]\n"
    "ds_read_b64_tr_b16 v[100:101], %[vb1] offset:30080\n"
    "ds_read_b64_tr_b16 v[102:103], %[vb1] offset:30336\n"
    "v_mfma_f32_16x16x32_bf16 v[148:151], v[76:79], v[68:71], v[148:151]\n"
    "ds_read_b64_tr_b16 v[104:105], %[vb2] offset:30080\n"
    "ds_read_b64_tr_b16 v[106:107], %[vb2] offset:30336\n"
    "v_mfma_f32_16x16x32_bf16 v[136:139], v[80:83], v[64:67], v[136:139]\n"
    "ds_read_b64_tr_b16 v[108:109], %[vb3] offset:30080\n"
    "ds_read_b64_tr_b16 v[110:111], %[vb3] offset:30336\n"
    "v_mfma_f32_16x16x32_bf16 v[152:155], v[80:83], v[68:71], v[152:155]\n"
    "v_add_u32 %[va], %[sf], %[va]\n"
    "v_mfma_f32_16x16x32_bf16 v[140:143], v[84:87], v[64:67], v[140:143]\n"
    "v_add_u32 %[vb0], %[sf], %[vb0]\n"
    "v_mfma_f32_16x16x32_bf16 v[156:159], v[84:87], v[68:71], v[156:159]\n"
    "v_add_u32 %[vb1], %[sf], %[vb1]\n"
    "v_mfma_f32_16x16x32_bf16 v[160:163], v[112:115], v[64:67], v[160:163]\n"
    "v_add_u32 %[vb2], %[sf], %[vb2]\n"
    "v_mfma_f32_16x16x32_bf16 v[164:167], v[112:115], v[68:71], v[164:167]\n"
    "v_add_u32 %[vb3], %[sf], %[vb3]\n"
    "v_mfma_f32_16x16x32_bf16 v[168:171], v[64:67], v[64:67], v[168:171]\n"
    "v_mfma_f32_16x16x32_bf16 v[172:175], v[68:71], v[68:71], v[172:175]\n"
    "v_mfma_f32_16x16x32_bf16 v[176:179], v[72:75], v[112:115], v[176:179]\n"
    "s_waitcnt lgkmcnt(0)\n"
    "s_barrier\n"
    "v_mfma_f32_16x16x32_bf16 v[128:131], v[96:99], v[88:91], v[128:131]\n"
    "ds_read_b128 v[64:67], %[va] offset:0\n"
    "ds_read_b128 v[68:71], %[va] offset:128\n"
    "v_mfma_f32_16x16x32_bf16 v[144:147], v[96:99], v[92:95], v[144:147]\n"
    "ds_read_b64_tr_b16 v[72:73], %[vb0] offset:0\n"
    "ds_read_b64_tr_b16 v[74:75], %[vb0] offset:256\n"
    "v_mfma_f32_16x16x32_bf16 v[132:135], v[100:103], v[88:91], v[132:135]\n"
    "ds_read_b64_tr_b16 v[76:77], %[vb1] offset:0\n"
    "ds_read_b64_tr_b16 v[78:79], %[vb1] offset:256\n"
    "v_mfma_f32_16x16x32_bf16 v[148:151], v[100:103], v[92:95], v[148:151]\n"
    "ds_read_b64_tr_b16 v[80:81], %[vb2] offset:0\n"
    "ds_read_b64_tr_b16 v[82:83], %[vb2] offset:256\n"
    "v_mfma_f32_16x16x32_bf16 v[136:139], v[104:107], v[88:91], v[136:139]\n"
    "ds_read_b64_tr_b16 v[84:85], %[vb3] offset:0\n"
    "ds_read_b64_tr_b16 v[86:87], %[vb3] offset:256\n"
    "v_mfma_f32_16x16x32_bf16 v[152:155], v[104:107], v[92:95], v[152:155]\n"
    "s_sub_u32 %[sf], 0, %[sf]\n"
    "v_mfma_f32_16x16x32_bf16 v[140:143], v[108:111], v[88:91], v[140:143]\n"
    "s_sub_u32 %[sc], %[sc], 1\n"
    "v_mfma_f32_16x16x32_bf16 v[156:159], v[108:111], v[92:95], v[156:159]\n"
    "v_mfma_f32_16x16x32_bf16 v[160:163], v[112:115], v[88:91], v[160:163]\n"
    "v_mfma_f32_16x16x32_bf16 v[164:167], v[112:115], v[92:95], v[164:167]\n"
    "v_mfma_f32_16x16x32_bf16 v[168:171], v[88:91], v[88:91], v[168:171]\n"
    "v_mfma_f32_16x16x32_bf16 v[172:175], v[92:95], v[92:95], v[172:175]\n"
    "v_mfma_f32_16x16x32_bf16 v[176:179], v[96:99], v[112:115], v[176:179]\n"
    "s_waitcnt lgkmcnt(0)\n"
    "s_cbranch_scc0 1b\n"
    "s_nop 7\n"
        : "=&{v[128:159]}"(acc), "=&{v[160:163]}"(rsum[0]), "=&{v[164:167]}"(rsum[1]), "=&{v[168:171]}"(rsq[0]),
          "=&{v[172:175]}"(rsq[1]), "=&{v[176:179]}"(csum), [va] "+&v"(va), [vb0] "+&v"(vb0), [vb1] "+&v"(vb1), [vb2] "+&v"(vb2),
          [vb3] "+&v"(vb3), [sf] "=&s"(sf), [sc] "=&s"(sc)
        : [ft] "v"(tagv + LV_U), [ws] "s"(ws), [fo] "s"(flag_at(LV_U, pm * 16 + un)), [hk] "s"(wave ? -1 : 4)
        : "memory", "scc", "v64", "v65", "v66", "v67", "v68", "v69", "v70", "v71", "v72", "v73", "v74", "v75", "v76", "v77", "v78", "v79", "v80", "v81", "v82", "v83", "v84", "v85", "v86", "v87", "v88", "v89", "v90", "v91", "v92", "v93", "v94", "v95", "v96", "v97", "v98", "v99", "v100", "v101", "v102", "v103", "v104", "v105", "v106", "v107", "v108", "v109", "v110", "v111", "v112", "v113", "v114", "v115");
    STAMP(3);
    LANES();
    if (mm == 0) *L<f32x4>(L_CS1 + wave * 64 + g * 16) = csum;
    u32x4 xw[2][2];
#pragma unroll
    for (int m = 0; m < 2; ++m)
#pragma unroll
      for (int f = 0; f < 2; ++f)
        xw[m][f] = ld16<0>(mk_rsrc(x, S * D * 2), (m * 16 + mm) * 2048 + f * 64 + (g & 1) * 32 + (g >> 1) * 16, (pm * 128 + wave * 32) * 2048 + pn * 128);
    LBAR();
    f32x4 cw[4];
#pragma unroll
    for (int f = 0; f < 4; ++f) cw[f] = *L<f32x4>(L_CS1 + (f ^ wave) * 64 + g * 16);
    f32x2 nr[2];
    const bool up = pn & 4;
    u32x4 oe[2];
#pragma unroll
    for (int m = 0; m < 2; ++m) {
      const float dq = __int_as_float(__builtin_amdgcn_ds_bpermute((mm + 16 * q) * 4, __float_as_int(rsq[m][p])));
      const float mu = rsum[m][0] * (1.0f / D);
      const float var = fmaxf(dq * (1.0f / D) - mu * mu, 0.f);
      const float r = __builtin_amdgcn_rsqf(var + EPS);
      const float rr = r * __builtin_amdgcn_rsqf(var * r * r + EPS);
      const int rl = wave * 32 + m * 16 + mm;
      if (g == 0) *L<f32x2>(L_ST1 + rl * 8) = (f32x2){-mu * r, r};
      const float k0r = GK0 * rr, k1r = k0r * 0.044715f * rr * rr, ir = __builtin_amdgcn_rcpf(rr);
      u32x2 pk[4];
#pragma unroll
      for (int f = 0; f < 4; ++f)
        pk[f] = (u32x2){pack2(gelu2(fma2(lo2(cw[f]), splat(-mu), pair_at(acc, 16 * m + 4 * f)), k0r, k1r, ir)),
                        pack2(gelu2(fma2(hi2(cw[f]), splat(-mu), pair_at(acc, 16 * m + 4 * f + 2)), k0r, k1r, ir))};
#pragma unroll
      for (int f = 0; f < 4; f += 2) {
        const u32x4 o = swap_pair(pk[f], pk[f + 1]);
        const unsigned co = ((((f + (g & 1)) ^ wave) << 1) + (g >> 1)) * HC + mm * 16;
        st16<AUX_SC1>(rw, co, WS_HS + (pm * 16 + pn) * 8 * HC + (wave * 32 + m * 16 + (up ? 0 : 8)) * 16, o);
        if (m == 0 || !up) oe[f >> 1] = o;
      }
      nr[m] = (f32x2){-mu * r, r};
    }
    if (wave == (up ? 0 : 3)) {
#pragma unroll
      for (int f = 0; f < 2; ++f)
        st16<AUX_SC1>(rw, (mm < 8) == up ? ((((2 * f + (g & 1)) ^ wave) << 1) + (g >> 1)) * HC + mm * 16 : WS_END,
                      WS_HS + (((pm + (up ? 15 : 1)) & 15) * 16 + pn) * 8 * HC + (up ? 2048 : -128), pm == (up ? 0 : 15) ? (u32x4){0u, 0u, 0u, 0u} : oe[f]);
    }
    FENCE();
#pragma unroll
    for (int m = 0; m < 2; ++m)
#pragma unroll
      for (int f = 0; f < 2; ++f) {
        const u32x4 w = swap_pair((u32x2){xw[m][f][0], xw[m][f][1]}, (u32x2){xw[m][f][2], xw[m][f][3]});
#pragma unroll
        for (int j = 0; j < 4; ++j) {
          const f32x2 a = fma2(bf_pair(w[j]), splat(nr[m].y), splat(nr[m].x));
          acc2[16 * m + 8 * f + 2 * j] = a.x, acc2[16 * m + 8 * f + 2 * j + 1] = a.y;
        }
      }
  }
  STAMP(4);
  f32x32 cacc;
#define CONV_ADDR() const unsigned vr = cvb + 480 - 32 * mm + 16 * g, vt0 = cvb + ((mm & 1) ? CV_K0 - 30 : CV_K1 - 28) + 2 * mm + 16 * g
  {
    LANES();
    CONV_ADDR();
    u32x4 dg;
#pragma unroll
    for (int j = 0; j < 4; ++j) dg[j] = 8 * g + 2 * j == 15 - mm ? dbits : 8 * g + 2 * j + 1 == 15 - mm ? dbits << 16 : 0u;
    asm volatile(
        "s_barrier\n"
    "ds_read_b128 v[96:99], %[vr] offset:3584\n"
    "ds_read_b128 v[100:103], %[vr] offset:3072\n"
    "ds_read_b128 v[104:107], %[vr] offset:2560\n"
    "ds_read_b128 v[108:111], %[vr] offset:2048\n"
    "ds_read_b128 v[112:115], %[vr] offset:1536\n"
    "ds_read_b128 v[116:119], %[vr] offset:1024\n"
    "ds_read_b128 v[120:123], %[vr] offset:512\n"
    "ds_read_b128 v[124:127], %[vr] offset:0\n"
    "ds_read2_b32 v[64:65], %[vt0] offset0:0 offset1:1\n"
    "ds_read2_b32 v[66:67], %[vt0] offset0:2 offset1:3\n"
    "ds_read2_b32 v[68:69], %[vt0] offset0:128 offset1:129\n"
    "ds_read2_b32 v[70:71], %[vt0] offset0:130 offset1:131\n"
    "ds_read2_b32 v[72:73], %[vt1] offset0:0 offset1:1\n"
    "ds_read2_b32 v[74:75], %[vt1] offset0:2 offset1:3\n"
    "ds_read2_b32 v[76:77], %[vt1] offset0:128 offset1:129\n"
    "ds_read2_b32 v[78:79], %[vt1] offset0:130 offset1:131\n"
    "ds_read2_b32 v[80:81], %[vt2] offset0:0 offset1:1\n"
    "ds_read2_b32 v[82:83], %[vt2] offset0:2 offset1:3\n"
    "ds_read2_b32 v[84:85], %[vt2] offset0:128 offset1:129\n"
    "ds_read2_b32 v[86:87], %[vt2] offset0:130 offset1:131\n"
    "ds_read2_b32 v[88:89], %[vt3] offset0:0 offset1:1\n"
    "ds_read2_b32 v[90:91], %[vt3] offset0:2 offset1:3\n"
    "ds_read2_b32 v[92:93], %[vt3] offset0:128 offset1:129\n"
    "ds_read2_b32 v[94:95], %[vt3] offset0:130 offset1:131\n"
    "s_waitcnt lgkmcnt(15)\n"
    "v_mfma_f32_16x16x32_bf16 v[224:227], %[dg], v[96:99], 0\n"
    "v_mfma_f32_16x16x32_bf16 v[228:231], %[dg], v[100:103], 0\n"
    "v_mfma_f32_16x16x32_bf16 v[232:235], %[dg], v[104:107], 0\n"
    "v_mfma_f32_16x16x32_bf16 v[236:239], %[dg], v[108:111], 0\n"
    "v_mfma_f32_16x16x32_bf16 v[240:243], %[dg], v[112:115], 0\n"
    "v_mfma_f32_16x16x32_bf16 v[244:247], %[dg], v[116:119], 0\n"
    "v_mfma_f32_16x16x32_bf16 v[248:251], %[dg], v[120:123], 0\n"
    "v_mfma_f32_16x16x32_bf16 v[252:255], %[dg], v[124:127], 0\n"
    "s_waitcnt lgkmcnt(0)\n"
    "v_mfma_f32_16x16x32_bf16 v[224:227], v[64:67], v[96:99], v[224:227]\n"
    "ds_read_b128 v[160:163], %[vr] offset:3648\n"
    "v_mfma_f32_16x16x32_bf16 v[252:255], v[92:95], v[96:99], v[252:255]\n"
    "ds_read_b128 v[164:167], %[vr] offset:3136\n"
    "v_mfma_f32_16x16x32_bf16 v[228:231], v[64:67], v[100:103], v[228:231]\n"
    "ds_read_b128 v[168:171], %[vr] offset:2624\n"
    "v_mfma_f32_16x16x32_bf16 v[232:235], v[64:67], v[104:107], v[232:235]\n"
    "ds_read_b128 v[172:175], %[vr] offset:2112\n"
    "v_mfma_f32_16x16x32_bf16 v[236:239], v[64:67], v[108:111], v[236:239]\n"
    "ds_read_b128 v[176:179], %[vr] offset:1600\n"
    "v_mfma_f32_16x16x32_bf16 v[240:243], v[64:67], v[112:115], v[240:243]\n"
    "ds_read_b128 v[180:183], %[vr] offset:1088\n"
    "v_mfma_f32_16x16x32_bf16 v[244:247], v[64:67], v[116:119], v[244:247]\n"
    "ds_read_b128 v[184:187], %[vr] offset:576\n"
    "v_mfma_f32_16x16x32_bf16 v[248:251], v[64:67], v[120:123], v[248:251]\n"
    "ds_read_b128 v[188:191], %[vr] offset:64\n"
    "v_mfma_f32_16x16x32_bf16 v[252:255], v[64:67], v[124:127], v[252:255]\n"
    "ds_read2_b32 v[128:129], %[vt0] offset0:16 offset1:17\n"
    "v_mfma_f32_16x16x32_bf16 v[228:231], v[68:71], v[96:99], v[228:231]\n"
    "ds_read2_b32 v[130:131], %[vt0] offset0:18 offset1:19\n"
    "v_mfma_f32_16x16x32_bf16 v[232:235], v[68:71], v[100:103], v[232:235]\n"
    "ds_read2_b32 v[132:133], %[vt0] offset0:144 offset1:145\n"
    "v_mfma_f32_16x16x32_bf16 v[236:239], v[68:71], v[104:107], v[236:239]\n"
    "ds_read2_b32 v[134:135], %[vt0] offset0:146 offset1:147\n"
    "v_mfma_f32_16x16x32_bf16 v[240:243], v[68:71], v[108:111], v[240:243]\n"
    "ds_read2_b32 v[136:137], %[vt1] offset0:16 offset1:17\n"
    "v_mfma_f32_16x16x32_bf16 v[244:247], v[68:71], v[112:115], v[244:247]\n"
    "ds_read2_b32 v[138:139], %[vt1] offset0:18 offset1:19\n"
    "v_mfma_f32_16x16x32_bf16 v[248:251], v[68:71], v[116:119], v[248:251]\n"
    "ds_read2_b32 v[140:141], %[vt1] offset0:144 offset1:145\n"
    "v_mfma_f32_16x16x32_bf16 v[252:255], v[68:71], v[120:123], v[252:255]\n"
    "ds_read2_b32 v[142:143], %[vt1] offset0:146 offset1:147\n"
    "v_mfma_f32_16x16x32_bf16 v[232:235], v[72:75], v[96:99], v[232:235]\n"
    "ds_read2_b32 v[144:145], %[vt2] offset0:16 offset1:17\n"
    "v_mfma_f32_16x16x32_bf16 v[236:239], v[72:75], v[100:103], v[236:239]\n"
    "ds_read2_b32 v[146:147], %[vt2] offset0:18 offset1:19\n"
    "v_mfma_f32_16x16x32_bf16 v[240:243], v[72:75], v[104:107], v[240:243]\n"
    "ds_read2_b32 v[148:149], %[vt2] offset0:144 offset1:145\n"
    "v_mfma_f32_16x16x32_bf16 v[244:247], v[72:75], v[108:111], v[244:247]\n"
    "ds_read2_b32 v[150:151], %[vt2] offset0:146 offset1:147\n"
    "v_mfma_f32_16x16x32_bf16 v[248:251], v[72:75], v[112:115], v[248:251]\n"
    "ds_read2_b32 v[152:153], %[vt3] offset0:16 offset1:17\n"
    "v_mfma_f32_16x16x32_bf16 v[252:255], v[72:75], v[116:119], v[252:255]\n"
    "ds_read2_b32 v[154:155], %[vt3] offset0:18 offset1:19\n"
    "v_mfma_f32_16x16x32_bf16 v[236:239], v[76:79], v[96:99], v[236:239]\n"
    "ds_read2_b32 v[156:157], %[vt3] offset0:144 offset1:145\n"
    "v_mfma_f32_16x16x32_bf16 v[240:243], v[76:79], v[100:103], v[240:243]\n"
    "ds_read2_b32 v[158:159], %[vt3] offset0:146 offset1:147\n"
    "v_mfma_f32_16x16x32_bf16 v[244:247], v[76:79], v[104:107], v[244:247]\n"
    "v_mfma_f32_16x16x32_bf16 v[248:251], v[76:79], v[108:111], v[248:251]\n"
    "v_mfma_f32_16x16x32_bf16 v[252:255], v[76:79], v[112:115], v[252:255]\n"
    "v_mfma_f32_16x16x32_bf16 v[240:243], v[80:83], v[96:99], v[240:243]\n"
    "v_mfma_f32_16x16x32_bf16 v[244:247], v[80:83], v[100:103], v[244:247]\n"
    "s_waitcnt vmcnt(0)\n"
    "s_mov_b64 exec, 1\n"
    "ds_add_u32 %[ca], %[c1]\n"
    "s_mov_b64 exec, -1\n"
    "v_mfma_f32_16x16x32_bf16 v[248:251], v[80:83], v[104:107], v[248:251]\n"
    "v_mfma_f32_16x16x32_bf16 v[252:255], v[80:83], v[108:111], v[252:255]\n"
    "v_mfma_f32_16x16x32_bf16 v[244:247], v[84:87], v[96:99], v[244:247]\n"
    "v_mfma_f32_16x16x32_bf16 v[248:251], v[84:87], v[100:103], v[248:251]\n"
    "v_mfma_f32_16x16x32_bf16 v[252:255], v[84:87], v[104:107], v[252:255]\n"
    "v_mfma_f32_16x16x32_bf16 v[248:251], v[88:91], v[96:99], v[248:251]\n"
    "s_waitcnt lgkmcnt(0)\n"
    "v_mfma_f32_16x16x32_bf16 v[252:255], v[88:91], v[100:103], v[252:255]\n"
    "v_mfma_f32_16x16x32_bf16 v[224:227], v[128:131], v[160:163], v[224:227]\n"
    "ds_read_b128 v[96:99], %[vr] offset:3712\n"
    "v_mfma_f32_16x16x32_bf16 v[252:255], v[156:159], v[160:163], v[252:255]\n"
    "ds_read_b128 v[100:103], %[vr] offset:3200\n"
    "v_mfma_f32_16x16x32_bf16 v[228:231], v[128:131], v[164:167], v[228:231]\n"
    "ds_read_b128 v[104:107], %[vr] offset:2688\n"
    "v_mfma_f32_16x16x32_bf16 v[232:235], v[128:131], v[168:171], v[232:235]\n"
    "ds_read_b128 v[108:111], %[vr] offset:2176\n"
    "v_mfma_f32_16x16x32_bf16 v[236:239], v[128:131], v[172:175], v[236:239]\n"
    "ds_read_b128 v[112:115], %[vr] offset:1664\n"
    "v_mfma_f32_16x16x32_bf16 v[240:243], v[128:131], v[176:179], v[240:243]\n"
    "ds_read_b128 v[116:119], %[vr] offset:1152\n"
    "v_mfma_f32_16x16x32_bf16 v[244:247], v[128:131], v[180:183], v[244:247]\n"
    "ds_read_b128 v[120:123], %[vr] offset:640\n"
    "v_mfma_f32_16x16x32_bf16 v[248:251], v[128:131], v[184:187], v[248:251]\n"
    "ds_read_b128 v[124:127], %[vr] offset:128\n"
    "v_mfma_f32_16x16x32_bf16 v[252:255], v[128:131], v[188:191], v[252:255]\n"
    "ds_read2_b32 v[64:65], %[vt0] offset0:32 offset1:33\n"
    "v_mfma_f32_16x16x32_bf16 v[228:231], v[132:135], v[160:163], v[228:231]\n"
    "ds_read2_b32 v[66:67], %[vt0] offset0:34 offset1:35\n"
    "v_mfma_f32_16x16x32_bf16 v[232:235], v[132:135], v[164:167], v[232:235]\n"
    "ds_read2_b32 v[68:69], %[vt0] offset0:160 offset1:161\n"
    "v_mfma_f32_16x16x32_bf16 v[236:239], v[132:135], v[168:171], v[236:239]\n"
    "ds_read2_b32 v[70:71], %[vt0] offset0:162 offset1:163\n"
    "v_mfma_f32_16x16x32_bf16 v[240:243], v[132:135], v[172:175], v[240:243]\n"
    "ds_read2_b32 v[72:73], %[vt1] offset0:32 offset1:33\n"
    "v_mfma_f32_16x16x32_bf16 v[244:247], v[132:135], v[176:179], v[244:247]\n"
    "ds_read2_b32 v[74:75], %[vt1] offset0:34 offset1:35\n"
    "v_mfma_f32_16x16x32_bf16 v[248:251], v[132:135], v[180:183], v[248:251]\n"
    "ds_read2_b32 v[76:77], %[vt1] offset0:160 offset1:161\n"
    "v_mfma_f32_16x16x32_bf16 v[252:255], v[132:135], v[184:187], v[252:255]\n"
    "ds_read2_b32 v[78:79], %[vt1] offset0:162 offset1:163\n"
    "v_mfma_f32_16x16x32_bf16 v[232:235], v[136:139], v[160:163], v[232:235]\n"
    "ds_read2_b32 v[80:81], %[vt2] offset0:32 offset1:33\n"
    "v_mfma_f32_16x16x32_bf16 v[236:239], v[136:139], v[164:167], v[236:239]\n"
    "ds_read2_b32 v[82:83], %[vt2] offset0:34 offset1:35\n"
    "v_mfma_f32_16x16x32_bf16 v[240:243], v[136:139], v[168:171], v[240:243]\n"
    "ds_read2_b32 v[84:85], %[vt2] offset0:160 offset1:161\n"
    "v_mfma_f32_16x16x32_bf16 v[244:247], v[136:139], v[172:175], v[244:247]\n"
    "ds_read2_b32 v[86:87], %[vt2] offset0:162 offset1:163\n"
    "v_mfma_f32_16x16x32_bf16 v[248:251], v[136:139], v[176:179], v[248:251]\n"
    "ds_read2_b32 v[88:89], %[vt3] offset0:32 offset1:33\n"
    "v_mfma_f32_16x16x32_bf16 v[252:255], v[136:139], v[180:183], v[252:255]\n"
    "ds_read2_b32 v[90:91], %[vt3] offset0:34 offset1:35\n"
    "v_mfma_f32_16x16x32_bf16 v[236:239], v[140:143], v[160:163], v[236:239]\n"
    "ds_read2_b32 v[92:93], %[vt3] offset0:160 offset1:161\n"
    "v_mfma_f32_16x16x32_bf16 v[240:243], v[140:143], v[164:167], v[240:243]\n"
    "ds_read2_b32 v[94:95], %[vt3] offset0:162 offset1:163\n"
    "v_mfma_f32_16x16x32_bf16 v[244:247], v[140:143], v[168:171], v[244:247]\n"
    "v_mfma_f32_16x16x32_bf16 v[248:251], v[140:143], v[172:175], v[248:251]\n"
    "v_mfma_f32_16x16x32_bf16 v[252:255], v[140:143], v[176:179], v[252:255]\n"
    "v_mfma_f32_16x16x32_bf16 v[240:243], v[144:147], v[160:163], v[240:243]\n"
    "v_mfma_f32_16x16x32_bf16 v[244:247], v[144:147], v[164:167], v[244:247]\n"
    "v_mfma_f32_16x16x32_bf16 v[248:251], v[144:147], v[168:171], v[248:251]\n"
    "v_mfma_f32_16x16x32_bf16 v[252:255], v[144:147], v[172:175], v[252:255]\n"
    "v_mfma_f32_16x16x32_bf16 v[244:247], v[148:151], v[160:163], v[244:247]\n"
    "v_mfma_f32_16x16x32_bf16 v[248:251], v[148:151], v[164:167], v[248:251]\n"
    "v_mfma_f32_16x16x32_bf16 v[252:255], v[148:151], v[168:171], v[252:255]\n"
    "v_mfma_f32_16x16x32_bf16 v[248:251], v[152:155], v[160:163], v[248:251]\n"
    "s_waitcnt lgkmcnt(0)\n"
    "v_mfma_f32_16x16x32_bf16 v[252:255], v[152:155], v[164:167], v[252:255]\n"
    "v_mfma_f32_16x16x32_bf16 v[224:227], v[64:67], v[96:99], v[224:227]\n"
    "ds_read_b128 v[160:163], %[vr] offset:3776\n"
    "v_mfma_f32_16x16x32_bf16 v[252:255], v[92:95], v[96:99], v[252:255]\n"
    "ds_read_b128 v[164:167], %[vr] offset:3264\n"
    "v_mfma_f32_16x16x32_bf16 v[228:231], v[64:67], v[100:103], v[228:231]\n"
    "ds_read_b128 v[168:171], %[vr] offset:2752\n"
    "v_mfma_f32_16x16x32_bf16 v[232:235], v[64:67], v[104:107], v[232:235]\n"
    "ds_read_b128 v[172:175], %[vr] offset:2240\n"
    "v_mfma_f32_16x16x32_bf16 v[236:239], v[64:67], v[108:111], v[236:239]\n"
    "ds_read_b128 v[176:179], %[vr] offset:1728\n"
    "v_mfma_f32_16x16x32_bf16 v[240:243], v[64:67], v[112:115], v[240:243]\n"
    "ds_read_b128 v[180:183], %[vr] offset:1216\n"
    "v_mfma_f32_16x16x32_bf16 v[244:247], v[64:67], v[116:119], v[244:247]\n"
    "ds_read_b128 v[184:187], %[vr] offset:704\n"
    "v_mfma_f32_16x16x32_bf16 v[248:251], v[64:67], v[120:123], v[248:251]\n"
    "ds_read_b128 v[188:191], %[vr] offset:192\n"
    "v_mfma_f32_16x16x32_bf16 v[252:255], v[64:67], v[124:127], v[252:255]\n"
    "ds_read2_b32 v[128:129], %[vt0] offset0:48 offset1:49\n"
    "v_mfma_f32_16x16x32_bf16 v[228:231], v[68:71], v[96:99], v[228:231]\n"
    "ds_read2_b32 v[130:131], %[vt0] offset0:50 offset1:51\n"
    "v_mfma_f32_16x16x32_bf16 v[232:235], v[68:71], v[100:103], v[232:235]\n"
    "ds_read2_b32 v[132:133], %[vt0] offset0:176 offset1:177\n"
    "v_mfma_f32_16x16x32_bf16 v[236:239], v[68:71], v[104:107], v[236:239]\n"
    "ds_read2_b32 v[134:135], %[vt0] offset0:178 offset1:179\n"
    "v_mfma_f32_16x16x32_bf16 v[240:243], v[68:71], v[108:111], v[240:243]\n"
    "ds_read2_b32 v[136:137], %[vt1] offset0:48 offset1:49\n"
    "v_mfma_f32_16x16x32_bf16 v[244:247], v[68:71], v[112:115], v[244:247]\n"
    "ds_read2_b32 v[138:139], %[vt1] offset0:50 offset1:51\n"
    "v_mfma_f32_16x16x32_bf16 v[248:251], v[68:71], v[116:119], v[248:251]\n"
    "ds_read2_b32 v[140:141], %[vt1] offset0:176 offset1:177\n"
    "v_mfma_f32_16x16x32_bf16 v[252:255], v[68:71], v[120:123], v[252:255]\n"
    "ds_read2_b32 v[142:143], %[vt1] offset0:178 offset1:179\n"
    "v_mfma_f32_16x16x32_bf16 v[232:235], v[72:75], v[96:99], v[232:235]\n"
    "ds_read2_b32 v[144:145], %[vt2] offset0:48 offset1:49\n"
    "v_mfma_f32_16x16x32_bf16 v[236:239], v[72:75], v[100:103], v[236:239]\n"
    "ds_read2_b32 v[146:147], %[vt2] offset0:50 offset1:51\n"
    "v_mfma_f32_16x16x32_bf16 v[240:243], v[72:75], v[104:107], v[240:243]\n"
    "ds_read2_b32 v[148:149], %[vt2] offset0:176 offset1:177\n"
    "v_mfma_f32_16x16x32_bf16 v[244:247], v[72:75], v[108:111], v[244:247]\n"
    "ds_read2_b32 v[150:151], %[vt2] offset0:178 offset1:179\n"
    "v_mfma_f32_16x16x32_bf16 v[248:251], v[72:75], v[112:115], v[248:251]\n"
    "ds_read2_b32 v[152:153], %[vt3] offset0:48 offset1:49\n"
    "v_mfma_f32_16x16x32_bf16 v[252:255], v[72:75], v[116:119], v[252:255]\n"
    "ds_read2_b32 v[154:155], %[vt3] offset0:50 offset1:51\n"
    "v_mfma_f32_16x16x32_bf16 v[236:239], v[76:79], v[96:99], v[236:239]\n"
    "ds_read2_b32 v[156:157], %[vt3] offset0:176 offset1:177\n"
    "v_mfma_f32_16x16x32_bf16 v[240:243], v[76:79], v[100:103], v[240:243]\n"
    "ds_read2_b32 v[158:159], %[vt3] offset0:178 offset1:179\n"
    "v_mfma_f32_16x16x32_bf16 v[244:247], v[76:79], v[104:107], v[244:247]\n"
    "v_mfma_f32_16x16x32_bf16 v[248:251], v[76:79], v[108:111], v[248:251]\n"
    "v_mfma_f32_16x16x32_bf16 v[252:255], v[76:79], v[112:115], v[252:255]\n"
    "v_mfma_f32_16x16x32_bf16 v[240:243], v[80:83], v[96:99], v[240:243]\n"
    "v_mfma_f32_16x16x32_bf16 v[244:247], v[80:83], v[100:103], v[244:247]\n"
    "v_mfma_f32_16x16x32_bf16 v[248:251], v[80:83], v[104:107], v[248:251]\n"
    "v_mfma_f32_16x16x32_bf16 v[252:255], v[80:83], v[108:111], v[252:255]\n"
    "v_mfma_f32_16x16x32_bf16 v[244:247], v[84:87], v[96:99], v[244:247]\n"
    "v_mfma_f32_16x16x32_bf16 v[248:251], v[84:87], v[100:103], v[248:251]\n"
    "v_mfma_f32_16x16x32_bf16 v[252:255], v[84:87], v[104:107], v[252:255]\n"
    "v_mfma_f32_16x16x32_bf16 v[248:251], v[88:91], v[96:99], v[248:251]\n"
    "s_waitcnt lgkmcnt(0)\n"
    "v_mfma_f32_16x16x32_bf16 v[252:255], v[88:91], v[100:103], v[252:255]\n"
    "v_mfma_f32_16x16x32_bf16 v[224:227], v[128:131], v[160:163], v[224:227]\n"
    "v_mfma_f32_16x16x32_bf16 v[252:255], v[156:159], v[160:163], v[252:255]\n"
    "v_mfma_f32_16x16x32_bf16 v[228:231], v[128:131], v[164:167], v[228:231]\n"
    "v_mfma_f32_16x16x32_bf16 v[232:235], v[128:131], v[168:171], v[232:235]\n"
    "v_mfma_f32_16x16x32_bf16 v[236:239], v[128:131], v[172:175], v[236:239]\n"
    "v_mfma_f32_16x16x32_bf16 v[240:243], v[128:131], v[176:179], v[240:243]\n"
    "v_mfma_f32_16x16x32_bf16 v[244:247], v[128:131], v[180:183], v[244:247]\n"
    "v_mfma_f32_16x16x32_bf16 v[248:251], v[128:131], v[184:187], v[248:251]\n"
    "v_mfma_f32_16x16x32_bf16 v[252:255], v[128:131], v[188:191], v[252:255]\n"
    "v_mfma_f32_16x16x32_bf16 v[228:231], v[132:135], v[160:163], v[228:231]\n"
    "v_mfma_f32_16x16x32_bf16 v[232:235], v[132:135], v[164:167], v[232:235]\n"
    "v_mfma_f32_16x16x32_bf16 v[236:239], v[132:135], v[168:171], v[236:239]\n"
    "v_mfma_f32_16x16x32_bf16 v[240:243], v[132:135], v[172:175], v[240:243]\n"
    "v_mfma_f32_16x16x32_bf16 v[244:247], v[132:135], v[176:179], v[244:247]\n"
    "v_mfma_f32_16x16x32_bf16 v[248:251], v[132:135], v[180:183], v[248:251]\n"
    "v_mfma_f32_16x16x32_bf16 v[252:255], v[132:135], v[184:187], v[252:255]\n"
    "v_mfma_f32_16x16x32_bf16 v[232:235], v[136:139], v[160:163], v[232:235]\n"
    "v_mfma_f32_16x16x32_bf16 v[236:239], v[136:139], v[164:167], v[236:239]\n"
    "v_mfma_f32_16x16x32_bf16 v[240:243], v[136:139], v[168:171], v[240:243]\n"
    "v_mfma_f32_16x16x32_bf16 v[244:247], v[136:139], v[172:175], v[244:247]\n"
    "v_mfma_f32_16x16x32_bf16 v[248:251], v[136:139], v[176:179], v[248:251]\n"
    "v_mfma_f32_16x16x32_bf16 v[252:255], v[136:139], v[180:183], v[252:255]\n"
    "v_mfma_f32_16x16x32_bf16 v[236:239], v[140:143], v[160:163], v[236:239]\n"
    "v_mfma_f32_16x16x32_bf16 v[240:243], v[140:143], v[164:167], v[240:243]\n"
    "v_mfma_f32_16x16x32_bf16 v[244:247], v[140:143], v[168:171], v[244:247]\n"
    "v_mfma_f32_16x16x32_bf16 v[248:251], v[140:143], v[172:175], v[248:251]\n"
    "v_mfma_f32_16x16x32_bf16 v[252:255], v[140:143], v[176:179], v[252:255]\n"
    "v_mfma_f32_16x16x32_bf16 v[240:243], v[144:147], v[160:163], v[240:243]\n"
    "v_mfma_f32_16x16x32_bf16 v[244:247], v[144:147], v[164:167], v[244:247]\n"
    "v_mfma_f32_16x16x32_bf16 v[248:251], v[144:147], v[168:171], v[248:251]\n"
    "v_mfma_f32_16x16x32_bf16 v[252:255], v[144:147], v[172:175], v[252:255]\n"
    "v_mfma_f32_16x16x32_bf16 v[244:247], v[148:151], v[160:163], v[244:247]\n"
    "v_mfma_f32_16x16x32_bf16 v[248:251], v[148:151], v[164:167], v[248:251]\n"
    "v_mfma_f32_16x16x32_bf16 v[252:255], v[148:151], v[168:171], v[252:255]\n"
    "v_mfma_f32_16x16x32_bf16 v[248:251], v[152:155], v[160:163], v[248:251]\n"
    "v_mfma_f32_16x16x32_bf16 v[252:255], v[152:155], v[164:167], v[252:255]\n"
    "s_waitcnt lgkmcnt(0)\n"
    "s_nop 7\n"
        : "=&{v[224:255]}"(cacc)
        : [vr] "v"(vr), [vt0] "v"(vt0), [vt1] "v"(vt0 + 1024), [vt2] "v"(vt0 + 2048), [vt3] "v"(vt0 + 3072), [dg] "v"(dg), [ca] "v"(L_CNT + 8),
          [c1] "v"(1)
        : "memory", "v64", "v65", "v66", "v67", "v68", "v69", "v70", "v71", "v72", "v73", "v74", "v75", "v76", "v77", "v78", "v79", "v80", "v81", "v82", "v83", "v84", "v85", "v86", "v87", "v88", "v89", "v90", "v91", "v92", "v93", "v94", "v95", "v96", "v97", "v98", "v99", "v100", "v101", "v102", "v103", "v104", "v105", "v106", "v107", "v108", "v109", "v110", "v111", "v112", "v113", "v114", "v115", "v116", "v117", "v118", "v119", "v120", "v121", "v122", "v123", "v124", "v125", "v126", "v127", "v128", "v129", "v130", "v131", "v132", "v133", "v134", "v135", "v136", "v137", "v138", "v139", "v140", "v141", "v142", "v143", "v144", "v145", "v146", "v147", "v148", "v149", "v150", "v151", "v152", "v153", "v154", "v155", "v156", "v157", "v158", "v159", "v160", "v161", "v162", "v163", "v164", "v165", "v166", "v167", "v168", "v169", "v170", "v171", "v172", "v173", "v174", "v175", "v176", "v177", "v178", "v179", "v180", "v181", "v182", "v183", "v184", "v185", "v186", "v187", "v188", "v189", "v190", "v191");
  }
  STAMP(5);
  {
    LANES();
    unsigned kc = (8 * pm + 4 * j8) & 31, op = 0x20601070u >> (kc & 24) & 255, t;
    unsigned va = g * 2176 + (wave * 32 + mm) * 16 + op, vb0 = VB64, sf, sc;
    asm volatile("s_barrier" : "+v"(va), "+v"(vb0)::"memory");
    STAMP(6);
    asm volatile(
    "ds_read_b128 v[64:67], %[va] offset:0\n"
    "ds_read_b128 v[68:71], %[va] offset:256\n"
    "ds_read_b64_tr_b16 v[72:73], %[vb0] offset:0\n"
    "ds_read_b64_tr_b16 v[74:75], %[vb0] offset:256\n"
    "ds_read_b64_tr_b16 v[76:77], %[vb0] offset:32\n"
    "ds_read_b64_tr_b16 v[78:79], %[vb0] offset:288\n"
    "ds_read_b64_tr_b16 v[80:81], %[vb0] offset:64\n"
    "ds_read_b64_tr_b16 v[82:83], %[vb0] offset:320\n"
    "ds_read_b64_tr_b16 v[84:85], %[vb0] offset:96\n"
    "ds_read_b64_tr_b16 v[86:87], %[vb0] offset:352\n"
    "s_mov_b32 %[sf], 51712\n"
    "s_mov_b32 %[sc], 7\n"
    "s_waitcnt lgkmcnt(0)\n"
    "1:\n"
    "v_mfma_f32_16x16x32_bf16 v[192:195], v[72:75], v[64:67], v[192:195]\n"
    "ds_read_b128 v[88:91], %[va] offset:8704\n"
    "ds_read_b128 v[92:95], %[va] offset:8960\n"
    "v_mfma_f32_16x16x32_bf16 v[208:211], v[72:75], v[68:71], v[208:211]\n"
    "ds_read_b64_tr_b16 v[96:97], %[vb0] offset:4224\n"
    "ds_read_b64_tr_b16 v[98:99], %[vb0] offset:4480\n"
    "v_mfma_f32_16x16x32_bf16 v[196:199], v[76:79], v[64:67], v[196:199]\n"
    "ds_read_b64_tr_b16 v[100:101], %[vb0] offset:4256\n"
    "ds_read_b64_tr_b16 v[102:103], %[vb0] offset:4512\n"
    "v_mfma_f32_16x16x32_bf16 v[212:215], v[76:79], v[68:71], v[212:215]\n"
    "ds_read_b64_tr_b16 v[104:105], %[vb0] offset:4288\n"
    "ds_read_b64_tr_b16 v[106:107], %[vb0] offset:4544\n"
    "v_mfma_f32_16x16x32_bf16 v[200:203], v[80:83], v[64:67], v[200:203]\n"
    "ds_read_b64_tr_b16 v[108:109], %[vb0] offset:4320\n"
    "ds_read_b64_tr_b16 v[110:111], %[vb0] offset:4576\n"
    "v_mfma_f32_16x16x32_bf16 v[216:219], v[80:83], v[68:71], v[216:219]\n"
    "s_add_u32 %[kc], %[kc], 4\n"
    "v_mfma_f32_16x16x32_bf16 v[204:207], v[84:87], v[64:67], v[204:207]\n"
    "s_and_b32 %[t], %[kc], 24\n"
    "v_mfma_f32_16x16x32_bf16 v[220:223], v[84:87], v[68:71], v[220:223]\n"
    "s_lshr_b32 %[t], 0x20601070, %[t]\n"
    "s_waitcnt lgkmcnt(0)\n"
    "s_barrier\n"
    "v_mfma_f32_16x16x32_bf16 v[192:195], v[96:99], v[88:91], v[192:195]\n"
    "ds_read_b128 v[64:67], %[va] offset:25856\n"
    "ds_read_b128 v[68:71], %[va] offset:26112\n"
    "v_mfma_f32_16x16x32_bf16 v[208:211], v[96:99], v[92:95], v[208:211]\n"
    "ds_read_b64_tr_b16 v[72:73], %[vb0] offset:25856\n"
    "ds_read_b64_tr_b16 v[74:75], %[vb0] offset:26112\n"
    "v_mfma_f32_16x16x32_bf16 v[196:199], v[100:103], v[88:91], v[196:199]\n"
    "ds_read_b64_tr_b16 v[76:77], %[vb0] offset:25888\n"
    "ds_read_b64_tr_b16 v[78:79], %[vb0] offset:26144\n"
    "v_mfma_f32_16x16x32_bf16 v[212:215], v[100:103], v[92:95], v[212:215]\n"
    "ds_read_b64_tr_b16 v[80:81], %[vb0] offset:25920\n"
    "ds_read_b64_tr_b16 v[82:83], %[vb0] offset:26176\n"
    "v_mfma_f32_16x16x32_bf16 v[200:203], v[104:107], v[88:91], v[200:203]\n"
    "ds_read_b64_tr_b16 v[84:85], %[vb0] offset:25952\n"
    "ds_read_b64_tr_b16 v[86:87], %[vb0] offset:26208\n"
    "v_mfma_f32_16x16x32_bf16 v[216:219], v[104:107], v[92:95], v[216:219]\n"
    "s_and_b32 %[t], %[t], 255\n"
    "v_mfma_f32_16x16x32_bf16 v[204:207], v[108:111], v[88:91], v[204:207]\n"
    "s_sub_u32 %[op], %[sf], %[op]\n"
    "v_mfma_f32_16x16x32_bf16 v[220:223], v[108:111], v[92:95], v[220:223]\n"
    "s_add_u32 %[op], %[op], %[t]\n"
    "s_waitcnt lgkmcnt(0)\n"
    "v_mfma_f32_16x16x32_bf16 v[192:195], v[72:75], v[64:67], v[192:195]\n"
    "ds_read_b128 v[88:91], %[va] offset:34560\n"
    "ds_read_b128 v[92:95], %[va] offset:34816\n"
    "v_mfma_f32_16x16x32_bf16 v[208:211], v[72:75], v[68:71], v[208:211]\n"
    "ds_read_b64_tr_b16 v[96:97], %[vb0] offset:30080\n"
    "ds_read_b64_tr_b16 v[98:99], %[vb0] offset:30336\n"
    "v_mfma_f32_16x16x32_bf16 v[196:199], v[76:79], v[64:67], v[196:199]\n"
    "ds_read_b64_tr_b16 v[100:101], %[vb0] offset:30112\n"
    "ds_read_b64_tr_b16 v[102:103], %[vb0] offset:30368\n"
    "v_mfma_f32_16x16x32_bf16 v[212:215], v[76:79], v[68:71], v[212:215]\n"
    "ds_read_b64_tr_b16 v[104:105], %[vb0] offset:30144\n"
    "ds_read_b64_tr_b16 v[106:107], %[vb0] offset:30400\n"
    "v_mfma_f32_16x16x32_bf16 v[200:203], v[80:83], v[64:67], v[200:203]\n"
    "ds_read_b64_tr_b16 v[108:109], %[vb0] offset:30176\n"
    "ds_read_b64_tr_b16 v[110:111], %[vb0] offset:30432\n"
    "v_mfma_f32_16x16x32_bf16 v[216:219], v[80:83], v[68:71], v[216:219]\n"
    "v_add_u32 %[va], %[op], %[va]\n"
    "v_mfma_f32_16x16x32_bf16 v[204:207], v[84:87], v[64:67], v[204:207]\n"
    "v_add_u32 %[vb0], %[sf], %[vb0]\n"
    "v_mfma_f32_16x16x32_bf16 v[220:223], v[84:87], v[68:71], v[220:223]\n"
    "s_mov_b32 %[op], %[t]\n"
    "s_waitcnt lgkmcnt(0)\n"
    "s_barrier\n"
    "v_mfma_f32_16x16x32_bf16 v[192:195], v[96:99], v[88:91], v[192:195]\n"
    "ds_read_b128 v[64:67], %[va] offset:0\n"
    "ds_read_b128 v[68:71], %[va] offset:256\n"
    "v_mfma_f32_16x16x32_bf16 v[208:211], v[96:99], v[92:95], v[208:211]\n"
    "ds_read_b64_tr_b16 v[72:73], %[vb0] offset:0\n"
    "ds_read_b64_tr_b16 v[74:75], %[vb0] offset:256\n"
    "v_mfma_f32_16x16x32_bf16 v[196:199], v[100:103], v[88:91], v[196:199]\n"
    "ds_read_b64_tr_b16 v[76:77], %[vb0] offset:32\n"
    "ds_read_b64_tr_b16 v[78:79], %[vb0] offset:288\n"
    "v_mfma_f32_16x16x32_bf16 v[212:215], v[100:103], v[92:95], v[212:215]\n"
    "ds_read_b64_tr_b16 v[80:81], %[vb0] offset:64\n"
    "ds_read_b64_tr_b16 v[82:83], %[vb0] offset:320\n"
    "v_mfma_f32_16x16x32_bf16 v[200:203], v[104:107], v[88:91], v[200:203]\n"
    "ds_read_b64_tr_b16 v[84:85], %[vb0] offset:96\n"
    "ds_read_b64_tr_b16 v[86:87], %[vb0] offset:352\n"
    "v_mfma_f32_16x16x32_bf16 v[216:219], v[104:107], v[92:95], v[216:219]\n"
    "s_sub_u32 %[sf], 0, %[sf]\n"
    "v_mfma_f32_16x16x32_bf16 v[204:207], v[108:111], v[88:91], v[204:207]\n"
    "s_sub_u32 %[sc], %[sc], 1\n"
    "v_mfma_f32_16x16x32_bf16 v[220:223], v[108:111], v[92:95], v[220:223]\n"
    "s_waitcnt lgkmcnt(0)\n"
    "s_cbranch_scc0 1b\n"
    "s_nop 7\n"
        : "+{v[192:223]}"(acc2), [va] "+&v"(va), [vb0] "+&v"(vb0), [sf] "=&s"(sf), [sc] "=&s"(sc), [kc] "+&s"(kc), [op] "+&s"(op), [t] "=&s"(t)
        :
        : "memory", "scc", "v64", "v65", "v66", "v67", "v68", "v69", "v70", "v71", "v72", "v73", "v74", "v75", "v76", "v77", "v78", "v79", "v80", "v81", "v82", "v83", "v84", "v85", "v86", "v87", "v88", "v89", "v90", "v91", "v92", "v93", "v94", "v95", "v96", "v97", "v98", "v99", "v100", "v101", "v102", "v103", "v104", "v105", "v106", "v107", "v108", "v109", "v110", "v111");
    STAMP(7);
    float sum[2], sq[2];
#pragma unroll
    for (int m = 0; m < 2; ++m) {
      u32x2 pk[4];
      f32x2 s = {0.f, 0.f}, s2 = {0.f, 0.f};
#pragma unroll
      for (int f = 0; f < 4; ++f) {
        const f32x2 v0 = pair_at(acc2, 16 * m + 4 * f), v1 = pair_at(acc2, 16 * m + 4 * f + 2);
        pk[f] = (u32x2){pack2(v0), pack2(v1)};
        s += v0 + v1;
        s2 = fma2(v0, v0, fma2(v1, v1, s2));
      }
#pragma unroll
      for (int f = 0; f < 4; f += 2)
        st16<AUX_SC1>(rw, (((f + (g & 1)) << 1) + (g >> 1)) * 2048 + (m * 16 + mm) * 16, WS_X1 + pm * 262144 + pn * 16384 + wave * 512,
                      swap_pair(pk[f], pk[f + 1]));
      sum[m] = s.x + s.y;
      sq[m] = s2.x + s2.y;
    }
    __builtin_amdgcn_raw_buffer_store_b32(__float_as_uint(red32(red16(sum[0], sum[1]), red16(sq[0], sq[1]))), rw, lane * 4,
                                          WS_ST + (pm * 16 + pn) * 1024 + wave * 256, AUX_SC1);
  }
  STAMP(8);
  const int wr = wave >> 1, wc = wave & 1;
  {
    LANES();
    CONV_ADDR();
    asm volatile(
    "ds_read_b128 v[96:99], %[vr] offset:3840\n"
    "ds_read_b128 v[100:103], %[vr] offset:3328\n"
    "ds_read_b128 v[104:107], %[vr] offset:2816\n"
    "ds_read_b128 v[108:111], %[vr] offset:2304\n"
    "ds_read_b128 v[112:115], %[vr] offset:1792\n"
    "ds_read_b128 v[116:119], %[vr] offset:1280\n"
    "ds_read_b128 v[120:123], %[vr] offset:768\n"
    "ds_read_b128 v[124:127], %[vr] offset:256\n"
    "ds_read2_b32 v[64:65], %[vt0] offset0:64 offset1:65\n"
    "ds_read2_b32 v[66:67], %[vt0] offset0:66 offset1:67\n"
    "ds_read2_b32 v[68:69], %[vt0] offset0:192 offset1:193\n"
    "ds_read2_b32 v[70:71], %[vt0] offset0:194 offset1:195\n"
    "ds_read2_b32 v[72:73], %[vt1] offset0:64 offset1:65\n"
    "ds_read2_b32 v[74:75], %[vt1] offset0:66 offset1:67\n"
    "ds_read2_b32 v[76:77], %[vt1] offset0:192 offset1:193\n"
    "ds_read2_b32 v[78:79], %[vt1] offset0:194 offset1:195\n"
    "ds_read2_b32 v[80:81], %[vt2] offset0:64 offset1:65\n"
    "ds_read2_b32 v[82:83], %[vt2] offset0:66 offset1:67\n"
    "ds_read2_b32 v[84:85], %[vt2] offset0:192 offset1:193\n"
    "ds_read2_b32 v[86:87], %[vt2] offset0:194 offset1:195\n"
    "ds_read2_b32 v[88:89], %[vt3] offset0:64 offset1:65\n"
    "ds_read2_b32 v[90:91], %[vt3] offset0:66 offset1:67\n"
    "ds_read2_b32 v[92:93], %[vt3] offset0:192 offset1:193\n"
    "ds_read2_b32 v[94:95], %[vt3] offset0:194 offset1:195\n"
    "s_waitcnt lgkmcnt(0)\n"
    "v_mfma_f32_16x16x32_bf16 v[224:227], v[64:67], v[96:99], v[224:227]\n"
    "ds_read_b128 v[160:163], %[vr] offset:3904\n"
    "v_mfma_f32_16x16x32_bf16 v[252:255], v[92:95], v[96:99], v[252:255]\n"
    "ds_read_b128 v[164:167], %[vr] offset:3392\n"
    "v_mfma_f32_16x16x32_bf16 v[228:231], v[64:67], v[100:103], v[228:231]\n"
    "ds_read_b128 v[168:171], %[vr] offset:2880\n"
    "v_mfma_f32_16x16x32_bf16 v[232:235], v[64:67], v[104:107], v[232:235]\n"
    "ds_read_b128 v[172:175], %[vr] offset:2368\n"
    "v_mfma_f32_16x16x32_bf16 v[236:239], v[64:67], v[108:111], v[236:239]\n"
    "ds_read_b128 v[176:179], %[vr] offset:1856\n"
    "v_mfma_f32_16x16x32_bf16 v[240:243], v[64:67], v[112:115], v[240:243]\n"
    "ds_read_b128 v[180:183], %[vr] offset:1344\n"
    "v_mfma_f32_16x16x32_bf16 v[244:247], v[64:67], v[116:119], v[244:247]\n"
    "ds_read_b128 v[184:187], %[vr] offset:832\n"
    "v_mfma_f32_16x16x32_bf16 v[248:251], v[64:67], v[120:123], v[248:251]\n"
    "ds_read_b128 v[188:191], %[vr] offset:320\n"
    "v_mfma_f32_16x16x32_bf16 v[252:255], v[64:67], v[124:127], v[252:255]\n"
    "ds_read2_b32 v[128:129], %[vt0] offset0:80 offset1:81\n"
    "v_mfma_f32_16x16x32_bf16 v[228:231], v[68:71], v[96:99], v[228:231]\n"
    "ds_read2_b32 v[130:131], %[vt0] offset0:82 offset1:83\n"
    "v_mfma_f32_16x16x32_bf16 v[232:235], v[68:71], v[100:103], v[232:235]\n"
    "ds_read2_b32 v[132:133], %[vt0] offset0:208 offset1:209\n"
    "v_mfma_f32_16x16x32_bf16 v[236:239], v[68:71], v[104:107], v[236:239]\n"
    "ds_read2_b32 v[134:135], %[vt0] offset0:210 offset1:211\n"
    "v_mfma_f32_16x16x32_bf16 v[240:243], v[68:71], v[108:111], v[240:243]\n"
    "ds_read2_b32 v[136:137], %[vt1] offset0:80 offset1:81\n"
    "v_mfma_f32_16x16x32_bf16 v[244:247], v[68:71], v[112:115], v[244:247]\n"
    "ds_read2_b32 v[138:139], %[vt1] offset0:82 offset1:83\n"
    "v_mfma_f32_16x16x32_bf16 v[248:251], v[68:71], v[116:119], v[248:251]\n"
    "ds_read2_b32 v[140:141], %[vt1] offset0:208 offset1:209\n"
    "v_mfma_f32_16x16x32_bf16 v[252:255], v[68:71], v[120:123], v[252:255]\n"
    "ds_read2_b32 v[142:143], %[vt1] offset0:210 offset1:211\n"
    "v_mfma_f32_16x16x32_bf16 v[232:235], v[72:75], v[96:99], v[232:235]\n"
    "ds_read2_b32 v[144:145], %[vt2] offset0:80 offset1:81\n"
    "v_mfma_f32_16x16x32_bf16 v[236:239], v[72:75], v[100:103], v[236:239]\n"
    "ds_read2_b32 v[146:147], %[vt2] offset0:82 offset1:83\n"
    "v_mfma_f32_16x16x32_bf16 v[240:243], v[72:75], v[104:107], v[240:243]\n"
    "ds_read2_b32 v[148:149], %[vt2] offset0:208 offset1:209\n"
    "v_mfma_f32_16x16x32_bf16 v[244:247], v[72:75], v[108:111], v[244:247]\n"
    "ds_read2_b32 v[150:151], %[vt2] offset0:210 offset1:211\n"
    "v_mfma_f32_16x16x32_bf16 v[248:251], v[72:75], v[112:115], v[248:251]\n"
    "ds_read2_b32 v[152:153], %[vt3] offset0:80 offset1:81\n"
    "v_mfma_f32_16x16x32_bf16 v[252:255], v[72:75], v[116:119], v[252:255]\n"
    "ds_read2_b32 v[154:155], %[vt3] offset0:82 offset1:83\n"
    "v_mfma_f32_16x16x32_bf16 v[236:239], v[76:79], v[96:99], v[236:239]\n"
    "ds_read2_b32 v[156:157], %[vt3] offset0:208 offset1:209\n"
    "v_mfma_f32_16x16x32_bf16 v[240:243], v[76:79], v[100:103], v[240:243]\n"
    "ds_read2_b32 v[158:159], %[vt3] offset0:210 offset1:211\n"
    "v_mfma_f32_16x16x32_bf16 v[244:247], v[76:79], v[104:107], v[244:247]\n"
    "v_mfma_f32_16x16x32_bf16 v[248:251], v[76:79], v[108:111], v[248:251]\n"
    "v_mfma_f32_16x16x32_bf16 v[252:255], v[76:79], v[112:115], v[252:255]\n"
    "v_mfma_f32_16x16x32_bf16 v[240:243], v[80:83], v[96:99], v[240:243]\n"
    "v_mfma_f32_16x16x32_bf16 v[244:247], v[80:83], v[100:103], v[244:247]\n"
    "s_waitcnt vmcnt(0)\n"
    "s_mov_b64 exec, 1\n"
    "ds_add_u32 %[ca], %[c1]\n"
    "s_mov_b64 exec, -1\n"
    "v_mfma_f32_16x16x32_bf16 v[248:251], v[80:83], v[104:107], v[248:251]\n"
    "v_mfma_f32_16x16x32_bf16 v[252:255], v[80:83], v[108:111], v[252:255]\n"
    "v_mfma_f32_16x16x32_bf16 v[244:247], v[84:87], v[96:99], v[244:247]\n"
    "v_mfma_f32_16x16x32_bf16 v[248:251], v[84:87], v[100:103], v[248:251]\n"
    "v_mfma_f32_16x16x32_bf16 v[252:255], v[84:87], v[104:107], v[252:255]\n"
    "v_mfma_f32_16x16x32_bf16 v[248:251], v[88:91], v[96:99], v[248:251]\n"
    "s_waitcnt lgkmcnt(0)\n"
    "v_mfma_f32_16x16x32_bf16 v[252:255], v[88:91], v[100:103], v[252:255]\n"
    "v_mfma_f32_16x16x32_bf16 v[224:227], v[128:131], v[160:163], v[224:227]\n"
    "ds_read_b128 v[96:99], %[vr] offset:3968\n"
    "v_mfma_f32_16x16x32_bf16 v[252:255], v[156:159], v[160:163], v[252:255]\n"
    "ds_read_b128 v[100:103], %[vr] offset:3456\n"
    "v_mfma_f32_16x16x32_bf16 v[228:231], v[128:131], v[164:167], v[228:231]\n"
    "ds_read_b128 v[104:107], %[vr] offset:2944\n"
    "v_mfma_f32_16x16x32_bf16 v[232:235], v[128:131], v[168:171], v[232:235]\n"
    "ds_read_b128 v[108:111], %[vr] offset:2432\n"
    "v_mfma_f32_16x16x32_bf16 v[236:239], v[128:131], v[172:175], v[236:239]\n"
    "ds_read_b128 v[112:115], %[vr] offset:1920\n"
    "v_mfma_f32_16x16x32_bf16 v[240:243], v[128:131], v[176:179], v[240:243]\n"
    "ds_read_b128 v[116:119], %[vr] offset:1408\n"
    "v_mfma_f32_16x16x32_bf16 v[244:247], v[128:131], v[180:183], v[244:247]\n"
    "ds_read_b128 v[120:123], %[vr] offset:896\n"
    "v_mfma_f32_16x16x32_bf16 v[248:251], v[128:131], v[184:187], v[248:251]\n"
    "ds_read_b128 v[124:127], %[vr] offset:384\n"
    "v_mfma_f32_16x16x32_bf16 v[252:255], v[128:131], v[188:191], v[252:255]\n"
    "ds_read2_b32 v[64:65], %[vt0] offset0:96 offset1:97\n"
    "v_mfma_f32_16x16x32_bf16 v[228:231], v[132:135], v[160:163], v[228:231]\n"
    "ds_read2_b32 v[66:67], %[vt0] offset0:98 offset1:99\n"
    "v_mfma_f32_16x16x32_bf16 v[232:235], v[132:135], v[164:167], v[232:235]\n"
    "ds_read2_b32 v[68:69], %[vt0] offset0:224 offset1:225\n"
    "v_mfma_f32_16x16x32_bf16 v[236:239], v[132:135], v[168:171], v[236:239]\n"
    "ds_read2_b32 v[70:71], %[vt0] offset0:226 offset1:227\n"
    "v_mfma_f32_16x16x32_bf16 v[240:243], v[132:135], v[172:175], v[240:243]\n"
    "ds_read2_b32 v[72:73], %[vt1] offset0:96 offset1:97\n"
    "v_mfma_f32_16x16x32_bf16 v[244:247], v[132:135], v[176:179], v[244:247]\n"
    "ds_read2_b32 v[74:75], %[vt1] offset0:98 offset1:99\n"
    "v_mfma_f32_16x16x32_bf16 v[248:251], v[132:135], v[180:183], v[248:251]\n"
    "ds_read2_b32 v[76:77], %[vt1] offset0:224 offset1:225\n"
    "v_mfma_f32_16x16x32_bf16 v[252:255], v[132:135], v[184:187], v[252:255]\n"
    "ds_read2_b32 v[78:79], %[vt1] offset0:226 offset1:227\n"
    "v_mfma_f32_16x16x32_bf16 v[232:235], v[136:139], v[160:163], v[232:235]\n"
    "ds_read2_b32 v[80:81], %[vt2] offset0:96 offset1:97\n"
    "v_mfma_f32_16x16x32_bf16 v[236:239], v[136:139], v[164:167], v[236:239]\n"
    "ds_read2_b32 v[82:83], %[vt2] offset0:98 offset1:99\n"
    "v_mfma_f32_16x16x32_bf16 v[240:243], v[136:139], v[168:171], v[240:243]\n"
    "ds_read2_b32 v[84:85], %[vt2] offset0:224 offset1:225\n"
    "v_mfma_f32_16x16x32_bf16 v[244:247], v[136:139], v[172:175], v[244:247]\n"
    "ds_read2_b32 v[86:87], %[vt2] offset0:226 offset1:227\n"
    "v_mfma_f32_16x16x32_bf16 v[248:251], v[136:139], v[176:179], v[248:251]\n"
    "ds_read2_b32 v[88:89], %[vt3] offset0:96 offset1:97\n"
    "v_mfma_f32_16x16x32_bf16 v[252:255], v[136:139], v[180:183], v[252:255]\n"
    "ds_read2_b32 v[90:91], %[vt3] offset0:98 offset1:99\n"
    "v_mfma_f32_16x16x32_bf16 v[236:239], v[140:143], v[160:163], v[236:239]\n"
    "ds_read2_b32 v[92:93], %[vt3] offset0:224 offset1:225\n"
    "v_mfma_f32_16x16x32_bf16 v[240:243], v[140:143], v[164:167], v[240:243]\n"
    "ds_read2_b32 v[94:95], %[vt3] offset0:226 offset1:227\n"
    "v_mfma_f32_16x16x32_bf16 v[244:247], v[140:143], v[168:171], v[244:247]\n"
    "v_mfma_f32_16x16x32_bf16 v[248:251], v[140:143], v[172:175], v[248:251]\n"
    "v_mfma_f32_16x16x32_bf16 v[252:255], v[140:143], v[176:179], v[252:255]\n"
    "v_mfma_f32_16x16x32_bf16 v[240:243], v[144:147], v[160:163], v[240:243]\n"
    "v_mfma_f32_16x16x32_bf16 v[244:247], v[144:147], v[164:167], v[244:247]\n"
    "v_mfma_f32_16x16x32_bf16 v[248:251], v[144:147], v[168:171], v[248:251]\n"
    "v_mfma_f32_16x16x32_bf16 v[252:255], v[144:147], v[172:175], v[252:255]\n"
    "v_mfma_f32_16x16x32_bf16 v[244:247], v[148:151], v[160:163], v[244:247]\n"
    "v_mfma_f32_16x16x32_bf16 v[248:251], v[148:151], v[164:167], v[248:251]\n"
    "v_mfma_f32_16x16x32_bf16 v[252:255], v[148:151], v[168:171], v[252:255]\n"
    "v_mfma_f32_16x16x32_bf16 v[248:251], v[152:155], v[160:163], v[248:251]\n"
    "s_waitcnt lgkmcnt(0)\n"
    "v_mfma_f32_16x16x32_bf16 v[252:255], v[152:155], v[164:167], v[252:255]\n"
    "v_mfma_f32_16x16x32_bf16 v[224:227], v[64:67], v[96:99], v[224:227]\n"
    "ds_read_b128 v[160:163], %[vr] offset:4032\n"
    "v_mfma_f32_16x16x32_bf16 v[252:255], v[92:95], v[96:99], v[252:255]\n"
    "ds_read_b128 v[164:167], %[vr] offset:3520\n"
    "v_mfma_f32_16x16x32_bf16 v[228:231], v[64:67], v[100:103], v[228:231]\n"
    "ds_read_b128 v[168:171], %[vr] offset:3008\n"
    "v_mfma_f32_16x16x32_bf16 v[232:235], v[64:67], v[104:107], v[232:235]\n"
    "ds_read_b128 v[172:175], %[vr] offset:2496\n"
    "v_mfma_f32_16x16x32_bf16 v[236:239], v[64:67], v[108:111], v[236:239]\n"
    "ds_read_b128 v[176:179], %[vr] offset:1984\n"
    "v_mfma_f32_16x16x32_bf16 v[240:243], v[64:67], v[112:115], v[240:243]\n"
    "ds_read_b128 v[180:183], %[vr] offset:1472\n"
    "v_mfma_f32_16x16x32_bf16 v[244:247], v[64:67], v[116:119], v[244:247]\n"
    "ds_read_b128 v[184:187], %[vr] offset:960\n"
    "v_mfma_f32_16x16x32_bf16 v[248:251], v[64:67], v[120:123], v[248:251]\n"
    "ds_read_b128 v[188:191], %[vr] offset:448\n"
    "v_mfma_f32_16x16x32_bf16 v[252:255], v[64:67], v[124:127], v[252:255]\n"
    "ds_read2_b32 v[128:129], %[vt0] offset0:112 offset1:113\n"
    "v_mfma_f32_16x16x32_bf16 v[228:231], v[68:71], v[96:99], v[228:231]\n"
    "ds_read2_b32 v[130:131], %[vt0] offset0:114 offset1:115\n"
    "v_mfma_f32_16x16x32_bf16 v[232:235], v[68:71], v[100:103], v[232:235]\n"
    "ds_read2_b32 v[132:133], %[vt0] offset0:240 offset1:241\n"
    "v_mfma_f32_16x16x32_bf16 v[236:239], v[68:71], v[104:107], v[236:239]\n"
    "ds_read2_b32 v[134:135], %[vt0] offset0:242 offset1:243\n"
    "v_mfma_f32_16x16x32_bf16 v[240:243], v[68:71], v[108:111], v[240:243]\n"
    "ds_read2_b32 v[136:137], %[vt1] offset0:112 offset1:113\n"
    "v_mfma_f32_16x16x32_bf16 v[244:247], v[68:71], v[112:115], v[244:247]\n"
    "ds_read2_b32 v[138:139], %[vt1] offset0:114 offset1:115\n"
    "v_mfma_f32_16x16x32_bf16 v[248:251], v[68:71], v[116:119], v[248:251]\n"
    "ds_read2_b32 v[140:141], %[vt1] offset0:240 offset1:241\n"
    "v_mfma_f32_16x16x32_bf16 v[252:255], v[68:71], v[120:123], v[252:255]\n"
    "ds_read2_b32 v[142:143], %[vt1] offset0:242 offset1:243\n"
    "v_mfma_f32_16x16x32_bf16 v[232:235], v[72:75], v[96:99], v[232:235]\n"
    "ds_read2_b32 v[144:145], %[vt2] offset0:112 offset1:113\n"
    "v_mfma_f32_16x16x32_bf16 v[236:239], v[72:75], v[100:103], v[236:239]\n"
    "ds_read2_b32 v[146:147], %[vt2] offset0:114 offset1:115\n"
    "v_mfma_f32_16x16x32_bf16 v[240:243], v[72:75], v[104:107], v[240:243]\n"
    "ds_read2_b32 v[148:149], %[vt2] offset0:240 offset1:241\n"
    "v_mfma_f32_16x16x32_bf16 v[244:247], v[72:75], v[108:111], v[244:247]\n"
    "ds_read2_b32 v[150:151], %[vt2] offset0:242 offset1:243\n"
    "v_mfma_f32_16x16x32_bf16 v[248:251], v[72:75], v[112:115], v[248:251]\n"
    "ds_read2_b32 v[152:153], %[vt3] offset0:112 offset1:113\n"
    "v_mfma_f32_16x16x32_bf16 v[252:255], v[72:75], v[116:119], v[252:255]\n"
    "ds_read2_b32 v[154:155], %[vt3] offset0:114 offset1:115\n"
    "v_mfma_f32_16x16x32_bf16 v[236:239], v[76:79], v[96:99], v[236:239]\n"
    "ds_read2_b32 v[156:157], %[vt3] offset0:240 offset1:241\n"
    "v_mfma_f32_16x16x32_bf16 v[240:243], v[76:79], v[100:103], v[240:243]\n"
    "ds_read2_b32 v[158:159], %[vt3] offset0:242 offset1:243\n"
    "v_mfma_f32_16x16x32_bf16 v[244:247], v[76:79], v[104:107], v[244:247]\n"
    "v_mfma_f32_16x16x32_bf16 v[248:251], v[76:79], v[108:111], v[248:251]\n"
    "v_mfma_f32_16x16x32_bf16 v[252:255], v[76:79], v[112:115], v[252:255]\n"
    "v_mfma_f32_16x16x32_bf16 v[240:243], v[80:83], v[96:99], v[240:243]\n"
    "v_mfma_f32_16x16x32_bf16 v[244:247], v[80:83], v[100:103], v[244:247]\n"
    "v_mfma_f32_16x16x32_bf16 v[248:251], v[80:83], v[104:107], v[248:251]\n"
    "v_mfma_f32_16x16x32_bf16 v[252:255], v[80:83], v[108:111], v[252:255]\n"
    "v_mfma_f32_16x16x32_bf16 v[244:247], v[84:87], v[96:99], v[244:247]\n"
    "v_mfma_f32_16x16x32_bf16 v[248:251], v[84:87], v[100:103], v[248:251]\n"
    "v_mfma_f32_16x16x32_bf16 v[252:255], v[84:87], v[104:107], v[252:255]\n"
    "v_mfma_f32_16x16x32_bf16 v[248:251], v[88:91], v[96:99], v[248:251]\n"
    "s_waitcnt lgkmcnt(0)\n"
    "v_mfma_f32_16x16x32_bf16 v[252:255], v[88:91], v[100:103], v[252:255]\n"
    "v_mfma_f32_16x16x32_bf16 v[224:227], v[128:131], v[160:163], v[224:227]\n"
    "v_mfma_f32_16x16x32_bf16 v[252:255], v[156:159], v[160:163], v[252:255]\n"
    "v_mfma_f32_16x16x32_bf16 v[228:231], v[128:131], v[164:167], v[228:231]\n"
    "v_mfma_f32_16x16x32_bf16 v[232:235], v[128:131], v[168:171], v[232:235]\n"
    "v_mfma_f32_16x16x32_bf16 v[236:239], v[128:131], v[172:175], v[236:239]\n"
    "v_mfma_f32_16x16x32_bf16 v[240:243], v[128:131], v[176:179], v[240:243]\n"
    "v_mfma_f32_16x16x32_bf16 v[244:247], v[128:131], v[180:183], v[244:247]\n"
    "v_mfma_f32_16x16x32_bf16 v[248:251], v[128:131], v[184:187], v[248:251]\n"
    "v_mfma_f32_16x16x32_bf16 v[252:255], v[128:131], v[188:191], v[252:255]\n"
    "v_mfma_f32_16x16x32_bf16 v[228:231], v[132:135], v[160:163], v[228:231]\n"
    "v_mfma_f32_16x16x32_bf16 v[232:235], v[132:135], v[164:167], v[232:235]\n"
    "v_mfma_f32_16x16x32_bf16 v[236:239], v[132:135], v[168:171], v[236:239]\n"
    "v_mfma_f32_16x16x32_bf16 v[240:243], v[132:135], v[172:175], v[240:243]\n"
    "v_mfma_f32_16x16x32_bf16 v[244:247], v[132:135], v[176:179], v[244:247]\n"
    "v_mfma_f32_16x16x32_bf16 v[248:251], v[132:135], v[180:183], v[248:251]\n"
    "v_mfma_f32_16x16x32_bf16 v[252:255], v[132:135], v[184:187], v[252:255]\n"
    "v_mfma_f32_16x16x32_bf16 v[232:235], v[136:139], v[160:163], v[232:235]\n"
    "v_mfma_f32_16x16x32_bf16 v[236:239], v[136:139], v[164:167], v[236:239]\n"
    "v_mfma_f32_16x16x32_bf16 v[240:243], v[136:139], v[168:171], v[240:243]\n"
    "v_mfma_f32_16x16x32_bf16 v[244:247], v[136:139], v[172:175], v[244:247]\n"
    "v_mfma_f32_16x16x32_bf16 v[248:251], v[136:139], v[176:179], v[248:251]\n"
    "v_mfma_f32_16x16x32_bf16 v[252:255], v[136:139], v[180:183], v[252:255]\n"
    "v_mfma_f32_16x16x32_bf16 v[236:239], v[140:143], v[160:163], v[236:239]\n"
    "v_mfma_f32_16x16x32_bf16 v[240:243], v[140:143], v[164:167], v[240:243]\n"
    "v_mfma_f32_16x16x32_bf16 v[244:247], v[140:143], v[168:171], v[244:247]\n"
    "v_mfma_f32_16x16x32_bf16 v[248:251], v[140:143], v[172:175], v[248:251]\n"
    "v_mfma_f32_16x16x32_bf16 v[252:255], v[140:143], v[176:179], v[252:255]\n"
    "v_mfma_f32_16x16x32_bf16 v[240:243], v[144:147], v[160:163], v[240:243]\n"
    "v_mfma_f32_16x16x32_bf16 v[244:247], v[144:147], v[164:167], v[244:247]\n"
    "v_mfma_f32_16x16x32_bf16 v[248:251], v[144:147], v[168:171], v[248:251]\n"
    "v_mfma_f32_16x16x32_bf16 v[252:255], v[144:147], v[172:175], v[252:255]\n"
    "v_mfma_f32_16x16x32_bf16 v[244:247], v[148:151], v[160:163], v[244:247]\n"
    "v_mfma_f32_16x16x32_bf16 v[248:251], v[148:151], v[164:167], v[248:251]\n"
    "v_mfma_f32_16x16x32_bf16 v[252:255], v[148:151], v[168:171], v[252:255]\n"
    "v_mfma_f32_16x16x32_bf16 v[248:251], v[152:155], v[160:163], v[248:251]\n"
    "v_mfma_f32_16x16x32_bf16 v[252:255], v[152:155], v[164:167], v[252:255]\n"
    "s_waitcnt lgkmcnt(0)\n"
    "s_nop 7\n"
        : "+{v[224:255]}"(cacc)
        : [vr] "v"(vr), [vt0] "v"(vt0), [vt1] "v"(vt0 + 1024), [vt2] "v"(vt0 + 2048), [vt3] "v"(vt0 + 3072), [ca] "v"(L_CNT + 8), [c1] "v"(1)
        : "memory", "v64", "v65", "v66", "v67", "v68", "v69", "v70", "v71", "v72", "v73", "v74", "v75", "v76", "v77", "v78", "v79", "v80", "v81", "v82", "v83", "v84", "v85", "v86", "v87", "v88", "v89", "v90", "v91", "v92", "v93", "v94", "v95", "v96", "v97", "v98", "v99", "v100", "v101", "v102", "v103", "v104", "v105", "v106", "v107", "v108", "v109", "v110", "v111", "v112", "v113", "v114", "v115", "v116", "v117", "v118", "v119", "v120", "v121", "v122", "v123", "v124", "v125", "v126", "v127", "v128", "v129", "v130", "v131", "v132", "v133", "v134", "v135", "v136", "v137", "v138", "v139", "v140", "v141", "v142", "v143", "v144", "v145", "v146", "v147", "v148", "v149", "v150", "v151", "v152", "v153", "v154", "v155", "v156", "v157", "v158", "v159", "v160", "v161", "v162", "v163", "v164", "v165", "v166", "v167", "v168", "v169", "v170", "v171", "v172", "v173", "v174", "v175", "v176", "v177", "v178", "v179", "v180", "v181", "v182", "v183", "v184", "v185", "v186", "v187", "v188", "v189", "v190", "v191");
#pragma unroll
    for (int jb = 0; jb < 8; ++jb)
      *L<u32x2>(cvb + 512 * jb + 32 * mm + 8 * g) = (u32x2){pack2(pair_at(cacc, 4 * jb)), pack2(pair_at(cacc, 4 * jb + 2))};
  }
  STAMP(9);
  {
    f32x16 acc[7];
    {
      LANES();
      unsigned ya = CV + q * CVW + (wave * 16 + g) * 64 + 8 * p;
      unsigned va0 = g * 2048 + (wr * 64 + mm) * 16, vb0 = 8192 + 7200 * (g & 1) + 448 * (q + 8 * (g >> 1)) + 224 * wc + 8 * p, va, vb, so, sc;
      asm volatile(
    "v_mov_b64 v[144:145], 0\n"
    "v_mov_b64 v[146:147], 0\n"
    "v_mov_b64 v[148:149], 0\n"
    "v_mov_b64 v[150:151], 0\n"
    "v_mov_b64 v[152:153], 0\n"
    "v_mov_b64 v[154:155], 0\n"
    "v_mov_b64 v[156:157], 0\n"
    "v_mov_b64 v[158:159], 0\n"
    "v_mov_b64 v[160:161], 0\n"
    "v_mov_b64 v[162:163], 0\n"
    "v_mov_b64 v[164:165], 0\n"
    "v_mov_b64 v[166:167], 0\n"
    "v_mov_b64 v[168:169], 0\n"
    "v_mov_b64 v[170:171], 0\n"
    "v_mov_b64 v[172:173], 0\n"
    "v_mov_b64 v[174:175], 0\n"
    "v_mov_b64 v[176:177], 0\n"
    "v_mov_b64 v[178:179], 0\n"
    "v_mov_b64 v[180:181], 0\n"
    "v_mov_b64 v[182:183], 0\n"
    "v_mov_b64 v[184:185], 0\n"
    "v_mov_b64 v[186:187], 0\n"
    "v_mov_b64 v[188:189], 0\n"
    "v_mov_b64 v[190:191], 0\n"
    "v_mov_b64 v[192:193], 0\n"
    "v_mov_b64 v[194:195], 0\n"
    "v_mov_b64 v[196:197], 0\n"
    "v_mov_b64 v[198:199], 0\n"
    "v_mov_b64 v[200:201], 0\n"
    "v_mov_b64 v[202:203], 0\n"
    "v_mov_b64 v[204:205], 0\n"
    "v_mov_b64 v[206:207], 0\n"
    "v_mov_b64 v[208:209], 0\n"
    "v_mov_b64 v[210:211], 0\n"
    "v_mov_b64 v[212:213], 0\n"
    "v_mov_b64 v[214:215], 0\n"
    "v_mov_b64 v[216:217], 0\n"
    "v_mov_b64 v[218:219], 0\n"
    "v_mov_b64 v[220:221], 0\n"
    "v_mov_b64 v[222:223], 0\n"
    "v_mov_b64 v[224:225], 0\n"
    "v_mov_b64 v[226:227], 0\n"
    "v_mov_b64 v[228:229], 0\n"
    "v_mov_b64 v[230:231], 0\n"
    "v_mov_b64 v[232:233], 0\n"
    "v_mov_b64 v[234:235], 0\n"
    "v_mov_b64 v[236:237], 0\n"
    "v_mov_b64 v[238:239], 0\n"
    "v_mov_b64 v[240:241], 0\n"
    "v_mov_b64 v[242:243], 0\n"
    "v_mov_b64 v[244:245], 0\n"
    "v_mov_b64 v[246:247], 0\n"
    "v_mov_b64 v[248:249], 0\n"
    "v_mov_b64 v[250:251], 0\n"
    "v_mov_b64 v[252:253], 0\n"
    "v_mov_b64 v[254:255], 0\n"
          "s_waitcnt lgkmcnt(0)\n\ts_barrier"
          : "=&{v[144:159]}"(acc[0]), "=&{v[160:175]}"(acc[1]), "=&{v[176:191]}"(acc[2]), "=&{v[192:207]}"(acc[3]), "=&{v[208:223]}"(acc[4]),
            "=&{v[224:239]}"(acc[5]), "=&{v[240:255]}"(acc[6]), "+v"(ya), "+v"(va0), "+v"(vb0)::"memory");
#pragma unroll
      for (int i = 0; i < 4; ++i) {
        const u32x2 lo = tr_read(ya + i * 256), hi = tr_read(ya + i * 256 + 32);
        st16<AUX_SC1>(rw, lane * 16 + i * 1024, WS_Y + (pn * 16 + pm) * 16384 + wave * 4096, (u32x4){lo.x, lo.y, hi.x, hi.y});
      }
      FENCE();
#pragma unroll
      for (int i = 0; i < 4; ++i)
        st16<AUX_SC1>(rw, lane * 16 + i * 1024, WS_XCH + (blockIdx.x * 4 + wave) * 4096, (u32x4){XCH_EMPTY, XCH_EMPTY, XCH_EMPTY, XCH_EMPTY});
      LBAR();
      STAMP(10);
      asm volatile(
    "v_mov_b32 %[va], %[va0]\n"
    "v_mov_b32 %[vb], %[vb0]\n"
    "s_mov_b32 %[so], 0\n"
    "ds_read_b128 v[56:59], %[va] offset:0\n"
    "ds_read_b64_tr_b16 v[72:73], %[vb] offset:0\n"
    "ds_read_b128 v[60:63], %[va] offset:256\n"
    "ds_read_b64_tr_b16 v[74:75], %[vb] offset:1792\n"
    "ds_read_b128 v[64:67], %[va] offset:512\n"
    "ds_read_b64_tr_b16 v[76:77], %[vb] offset:32\n"
    "ds_read_b128 v[68:71], %[va] offset:768\n"
    "ds_read_b64_tr_b16 v[78:79], %[vb] offset:1824\n"
    "ds_read_b64_tr_b16 v[80:81], %[vb] offset:64\n"
    "ds_read_b64_tr_b16 v[82:83], %[vb] offset:1856\n"
    "ds_read_b64_tr_b16 v[84:85], %[vb] offset:96\n"
    "ds_read_b64_tr_b16 v[86:87], %[vb] offset:1888\n"
    "ds_read_b64_tr_b16 v[88:89], %[vb] offset:128\n"
    "ds_read_b64_tr_b16 v[90:91], %[vb] offset:1920\n"
    "ds_read_b64_tr_b16 v[92:93], %[vb] offset:160\n"
    "ds_read_b64_tr_b16 v[94:95], %[vb] offset:1952\n"
    "ds_read_b64_tr_b16 v[96:97], %[vb] offset:192\n"
    "ds_read_b64_tr_b16 v[98:99], %[vb] offset:1984\n"
    "s_add_u32 %[so], %[so], 22560\n"
    "s_cmp_lg_u32 %[so], 157920\n"
    "s_cselect_b32 %[so], %[so], 0\n"
    "v_add_u32 %[va], %[so], %[va0]\n"
    "v_add_u32 %[vb], %[so], %[vb0]\n"
    "s_mov_b32 %[sc], 15\n"
    "s_waitcnt lgkmcnt(0)\n"
    "s_barrier\n"
    "1:\n"
    "v_mfma_f32_16x16x32_bf16 v[144:147], v[72:75], v[56:59], v[144:147]\n"
    "ds_read_b128 v[100:103], %[va] offset:0\n"
    "v_mfma_f32_16x16x32_bf16 v[148:151], v[72:75], v[60:63], v[148:151]\n"
    "ds_read_b64_tr_b16 v[116:117], %[vb] offset:0\n"
    "v_mfma_f32_16x16x32_bf16 v[152:155], v[72:75], v[64:67], v[152:155]\n"
    "ds_read_b128 v[104:107], %[va] offset:256\n"
    "v_mfma_f32_16x16x32_bf16 v[156:159], v[72:75], v[68:71], v[156:159]\n"
    "ds_read_b64_tr_b16 v[118:119], %[vb] offset:1792\n"
    "v_mfma_f32_16x16x32_bf16 v[160:163], v[76:79], v[56:59], v[160:163]\n"
    "ds_read_b128 v[108:111], %[va] offset:512\n"
    "v_mfma_f32_16x16x32_bf16 v[164:167], v[76:79], v[60:63], v[164:167]\n"
    "ds_read_b64_tr_b16 v[120:121], %[vb] offset:32\n"
    "v_mfma_f32_16x16x32_bf16 v[168:171], v[76:79], v[64:67], v[168:171]\n"
    "ds_read_b128 v[112:115], %[va] offset:768\n"
    "v_mfma_f32_16x16x32_bf16 v[172:175], v[76:79], v[68:71], v[172:175]\n"
    "ds_read_b64_tr_b16 v[122:123], %[vb] offset:1824\n"
    "v_mfma_f32_16x16x32_bf16 v[176:179], v[80:83], v[56:59], v[176:179]\n"
    "ds_read_b64_tr_b16 v[124:125], %[vb] offset:64\n"
    "v_mfma_f32_16x16x32_bf16 v[180:183], v[80:83], v[60:63], v[180:183]\n"
    "ds_read_b64_tr_b16 v[126:127], %[vb] offset:1856\n"
    "v_mfma_f32_16x16x32_bf16 v[184:187], v[80:83], v[64:67], v[184:187]\n"
    "ds_read_b64_tr_b16 v[128:129], %[vb] offset:96\n"
    "v_mfma_f32_16x16x32_bf16 v[188:191], v[80:83], v[68:71], v[188:191]\n"
    "ds_read_b64_tr_b16 v[130:131], %[vb] offset:1888\n"
    "v_mfma_f32_16x16x32_bf16 v[192:195], v[84:87], v[56:59], v[192:195]\n"
    "ds_read_b64_tr_b16 v[132:133], %[vb] offset:128\n"
    "v_mfma_f32_16x16x32_bf16 v[196:199], v[84:87], v[60:63], v[196:199]\n"
    "ds_read_b64_tr_b16 v[134:135], %[vb] offset:1920\n"
    "v_mfma_f32_16x16x32_bf16 v[200:203], v[84:87], v[64:67], v[200:203]\n"
    "ds_read_b64_tr_b16 v[136:137], %[vb] offset:160\n"
    "v_mfma_f32_16x16x32_bf16 v[204:207], v[84:87], v[68:71], v[204:207]\n"
    "ds_read_b64_tr_b16 v[138:139], %[vb] offset:1952\n"
    "v_mfma_f32_16x16x32_bf16 v[208:211], v[88:91], v[56:59], v[208:211]\n"
    "ds_read_b64_tr_b16 v[140:141], %[vb] offset:192\n"
    "v_mfma_f32_16x16x32_bf16 v[212:215], v[88:91], v[60:63], v[212:215]\n"
    "ds_read_b64_tr_b16 v[142:143], %[vb] offset:1984\n"
    "v_mfma_f32_16x16x32_bf16 v[216:219], v[88:91], v[64:67], v[216:219]\n"
    "s_add_u32 %[so], %[so], 22560\n"
    "v_mfma_f32_16x16x32_bf16 v[220:223], v[88:91], v[68:71], v[220:223]\n"
    "s_cmp_lg_u32 %[so], 157920\n"
    "v_mfma_f32_16x16x32_bf16 v[224:227], v[92:95], v[56:59], v[224:227]\n"
    "s_cselect_b32 %[so], %[so], 0\n"
    "v_mfma_f32_16x16x32_bf16 v[228:231], v[92:95], v[60:63], v[228:231]\n"
    "v_add_u32 %[va], %[so], %[va0]\n"
    "v_mfma_f32_16x16x32_bf16 v[232:235], v[92:95], v[64:67], v[232:235]\n"
    "v_add_u32 %[vb], %[so], %[vb0]\n"
    "v_mfma_f32_16x16x32_bf16 v[236:239], v[92:95], v[68:71], v[236:239]\n"
    "s_cmp_lg_u32 %[sc], 13\n"
    "v_mfma_f32_16x16x32_bf16 v[240:243], v[96:99], v[56:59], v[240:243]\n"
    "s_cbranch_scc1 2f\n"
    "s_waitcnt vmcnt(0)\n"
    "2:\n"
    "v_mfma_f32_16x16x32_bf16 v[244:247], v[96:99], v[60:63], v[244:247]\n"
    "v_mfma_f32_16x16x32_bf16 v[248:251], v[96:99], v[64:67], v[248:251]\n"
    "s_waitcnt lgkmcnt(0)\n"
    "v_mfma_f32_16x16x32_bf16 v[252:255], v[96:99], v[68:71], v[252:255]\n"
    "s_barrier\n"
    "v_mfma_f32_16x16x32_bf16 v[144:147], v[116:119], v[100:103], v[144:147]\n"
    "ds_read_b128 v[56:59], %[va] offset:0\n"
    "v_mfma_f32_16x16x32_bf16 v[148:151], v[116:119], v[104:107], v[148:151]\n"
    "ds_read_b64_tr_b16 v[72:73], %[vb] offset:0\n"
    "v_mfma_f32_16x16x32_bf16 v[152:155], v[116:119], v[108:111], v[152:155]\n"
    "ds_read_b128 v[60:63], %[va] offset:256\n"
    "v_mfma_f32_16x16x32_bf16 v[156:159], v[116:119], v[112:115], v[156:159]\n"
    "ds_read_b64_tr_b16 v[74:75], %[vb] offset:1792\n"
    "v_mfma_f32_16x16x32_bf16 v[160:163], v[120:123], v[100:103], v[160:163]\n"
    "ds_read_b128 v[64:67], %[va] offset:512\n"
    "v_mfma_f32_16x16x32_bf16 v[164:167], v[120:123], v[104:107], v[164:167]\n"
    "ds_read_b64_tr_b16 v[76:77], %[vb] offset:32\n"
    "v_mfma_f32_16x16x32_bf16 v[168:171], v[120:123], v[108:111], v[168:171]\n"
    "ds_read_b128 v[68:71], %[va] offset:768\n"
    "v_mfma_f32_16x16x32_bf16 v[172:175], v[120:123], v[112:115], v[172:175]\n"
    "ds_read_b64_tr_b16 v[78:79], %[vb] offset:1824\n"
    "v_mfma_f32_16x16x32_bf16 v[176:179], v[124:127], v[100:103], v[176:179]\n"
    "ds_read_b64_tr_b16 v[80:81], %[vb] offset:64\n"
    "v_mfma_f32_16x16x32_bf16 v[180:183], v[124:127], v[104:107], v[180:183]\n"
    "ds_read_b64_tr_b16 v[82:83], %[vb] offset:1856\n"
    "v_mfma_f32_16x16x32_bf16 v[184:187], v[124:127], v[108:111], v[184:187]\n"
    "ds_read_b64_tr_b16 v[84:85], %[vb] offset:96\n"
    "v_mfma_f32_16x16x32_bf16 v[188:191], v[124:127], v[112:115], v[188:191]\n"
    "ds_read_b64_tr_b16 v[86:87], %[vb] offset:1888\n"
    "v_mfma_f32_16x16x32_bf16 v[192:195], v[128:131], v[100:103], v[192:195]\n"
    "ds_read_b64_tr_b16 v[88:89], %[vb] offset:128\n"
    "v_mfma_f32_16x16x32_bf16 v[196:199], v[128:131], v[104:107], v[196:199]\n"
    "ds_read_b64_tr_b16 v[90:91], %[vb] offset:1920\n"
    "v_mfma_f32_16x16x32_bf16 v[200:203], v[128:131], v[108:111], v[200:203]\n"
    "ds_read_b64_tr_b16 v[92:93], %[vb] offset:160\n"
    "v_mfma_f32_16x16x32_bf16 v[204:207], v[128:131], v[112:115], v[204:207]\n"
    "ds_read_b64_tr_b16 v[94:95], %[vb] offset:1952\n"
    "v_mfma_f32_16x16x32_bf16 v[208:211], v[132:135], v[100:103], v[208:211]\n"
    "ds_read_b64_tr_b16 v[96:97], %[vb] offset:192\n"
    "v_mfma_f32_16x16x32_bf16 v[212:215], v[132:135], v[104:107], v[212:215]\n"
    "ds_read_b64_tr_b16 v[98:99], %[vb] offset:1984\n"
    "v_mfma_f32_16x16x32_bf16 v[216:219], v[132:135], v[108:111], v[216:219]\n"
    "s_add_u32 %[so], %[so], 22560\n"
    "v_mfma_f32_16x16x32_bf16 v[220:223], v[132:135], v[112:115], v[220:223]\n"
    "s_cmp_lg_u32 %[so], 157920\n"
    "v_mfma_f32_16x16x32_bf16 v[224:227], v[136:139], v[100:103], v[224:227]\n"
    "s_cselect_b32 %[so], %[so], 0\n"
    "v_mfma_f32_16x16x32_bf16 v[228:231], v[136:139], v[104:107], v[228:231]\n"
    "v_add_u32 %[va], %[so], %[va0]\n"
    "v_mfma_f32_16x16x32_bf16 v[232:235], v[136:139], v[108:111], v[232:235]\n"
    "v_add_u32 %[vb], %[so], %[vb0]\n"
    "v_mfma_f32_16x16x32_bf16 v[236:239], v[136:139], v[112:115], v[236:239]\n"
    "s_cmp_lg_u32 %[sc], %[hk]\n"
    "v_mfma_f32_16x16x32_bf16 v[240:243], v[140:143], v[100:103], v[240:243]\n"
    "s_cbranch_scc1 3f\n"
    "s_mov_b64 exec, 1\n"
    "buffer_store_dword %[ft], off, %[ws], %[fo] sc1\n"
    "s_mov_b64 exec, -1\n"
    "3:\n"
    "v_mfma_f32_16x16x32_bf16 v[244:247], v[140:143], v[104:107], v[244:247]\n"
    "s_sub_u32 %[sc], %[sc], 1\n"
    "v_mfma_f32_16x16x32_bf16 v[248:251], v[140:143], v[108:111], v[248:251]\n"
    "s_waitcnt lgkmcnt(0)\n"
    "v_mfma_f32_16x16x32_bf16 v[252:255], v[140:143], v[112:115], v[252:255]\n"
    "s_barrier\n"
    "s_cbranch_scc0 1b\n"
    "s_nop 7\n"
        : "+{v[144:159]}"(acc[0]), "+{v[160:175]}"(acc[1]), "+{v[176:191]}"(acc[2]), "+{v[192:207]}"(acc[3]), "+{v[208:223]}"(acc[4]),
          "+{v[224:239]}"(acc[5]), "+{v[240:255]}"(acc[6]), [va] "=&v"(va), [vb] "=&v"(vb), [so] "=&s"(so), [sc] "=&s"(sc)
        : [va0] "v"(va0), [vb0] "v"(vb0), [ft] "v"(tagv + LV_Y), [ws] "s"(ws), [fo] "s"(fo + LV_Y * 32768), [hk] "s"(wave ? -1 : 13)
        : "memory", "scc", "v56", "v57", "v58", "v59", "v60", "v61", "v62", "v63", "v64", "v65", "v66", "v67", "v68", "v69", "v70", "v71", "v72", "v73", "v74", "v75", "v76", "v77", "v78", "v79", "v80", "v81", "v82", "v83", "v84", "v85", "v86", "v87", "v88", "v89", "v90", "v91", "v92", "v93", "v94", "v95", "v96", "v97", "v98", "v99", "v100", "v101", "v102", "v103", "v104", "v105", "v106", "v107", "v108", "v109", "v110", "v111", "v112", "v113", "v114", "v115", "v116", "v117", "v118", "v119", "v120", "v121", "v122", "v123", "v124", "v125", "v126", "v127", "v128", "v129", "v130", "v131", "v132", "v133", "v134", "v135", "v136", "v137", "v138", "v139", "v140", "v141", "v142", "v143");
    }
    STAMP(11);
    LANES();
    float nmu[4], k0r[4], k1r[4], ir[4];
#pragma unroll
    for (int m = 0; m < 4; ++m) {
      const f32x2 c = *L<f32x2>(L3_ROW + (wr * 64 + m * 16 + mm) * 8);
      nmu[m] = c.x, k0r[m] = GK0 * c.y, k1r[m] = k0r[m] * 0.044715f * c.y * c.y, ir[m] = __builtin_amdgcn_rcpf(c.y);
    }
    f32x4 cw[7];
#pragma unroll
    for (int f = 0; f < 7; ++f) cw[f] = *L<f32x4>(L3_COL + (wc * 112 + f * 16 + 4 * g) * 4);
    const unsigned ao = (2 * wc + (g >> 1)) * 2048 + (wr * 64 + (g & 1) * 16 + mm) * 16;
#pragma unroll
    for (int f = 0; f < 7; ++f) {
      u32x2 pk[4];
#pragma unroll
      for (int m = 0; m < 4; ++m)
        pk[m] = (u32x2){pack2(gelu2(fma2(lo2(cw[f]), splat(nmu[m]), pair_at(acc[f], 4 * m)), k0r[m], k1r[m], ir[m])),
                        pack2(gelu2(fma2(hi2(cw[f]), splat(nmu[m]), pair_at(acc[f], 4 * m + 2)), k0r[m], k1r[m], ir[m]))};
#pragma unroll
      for (int m = 0; m < 4; m += 2) {
        const u32x4 o = swap_pair(pk[m], pk[m + 1]);
        st16<AUX_SC1>(rw, ao + m * 256, WS_H3 + ((pm * 16 + pn) * 7 + f) * 8192, o);
        *L<u32x4>(f * SL4 + ao + m * 256) = o;
      }
      FENCE();
      if (f == 3) {
        XSTAMP(1);
        wait_vm<4>();
        XSTAMP(2);
        if (lane == 0) __hip_atomic_fetch_add(L<unsigned>(L_CNT + 8), 1u, __ATOMIC_RELAXED, __HIP_MEMORY_SCOPE_WORKGROUP);
      }
      if (f == 5) *L<unsigned>(L_CNT + 4) = 1u;
      FENCE();
    }
    XSTAMP(3);
    wait_vm<6>();
  }
  STAMP(12);
  {
    LANES();
    f32x32 keep, send;
    unsigned sc;
    const unsigned vk = g * 2048 + (kh * 64 + wr * 32 + mm) * 16, vs = vk ^ 1024, vb = 8192 + 1056 * (q + 4 * (g & 1)) + 512 * (g >> 1) + 128 * wc + 8 * p;
    asm volatile(
        "s_waitcnt lgkmcnt(0)\n\ts_barrier\n"
    "ds_read_b128 v[128:131], %[vk0] offset:0\n"
    "ds_read_b64_tr_b16 v[144:145], %[vb0] offset:0\n"
    "ds_read_b128 v[132:135], %[vk0] offset:256\n"
    "ds_read_b64_tr_b16 v[146:147], %[vb0] offset:256\n"
    "ds_read_b128 v[136:139], %[vs0] offset:0\n"
    "ds_read_b64_tr_b16 v[148:149], %[vb0] offset:32\n"
    "ds_read_b128 v[140:143], %[vs0] offset:256\n"
    "ds_read_b64_tr_b16 v[150:151], %[vb0] offset:288\n"
    "ds_read_b64_tr_b16 v[152:153], %[vb0] offset:64\n"
    "ds_read_b64_tr_b16 v[154:155], %[vb0] offset:320\n"
    "ds_read_b64_tr_b16 v[156:157], %[vb0] offset:96\n"
    "ds_read_b64_tr_b16 v[158:159], %[vb0] offset:352\n"
    "s_cmp_lg_u32 %[hk], 6\n"
    "s_cbranch_scc1 4f\n"
    "s_mov_b64 exec, 1\n"
    "buffer_store_dword %[f1], off, %[ws], %[fp] sc1\n"
    "s_mov_b64 exec, -1\n"
    "4:\n"
    "v_mov_b64 v[192:193], 0\n"
    "v_mov_b64 v[194:195], 0\n"
    "v_mov_b64 v[196:197], 0\n"
    "v_mov_b64 v[198:199], 0\n"
    "v_mov_b64 v[200:201], 0\n"
    "v_mov_b64 v[202:203], 0\n"
    "v_mov_b64 v[204:205], 0\n"
    "v_mov_b64 v[206:207], 0\n"
    "v_mov_b64 v[208:209], 0\n"
    "v_mov_b64 v[210:211], 0\n"
    "v_mov_b64 v[212:213], 0\n"
    "v_mov_b64 v[214:215], 0\n"
    "v_mov_b64 v[216:217], 0\n"
    "v_mov_b64 v[218:219], 0\n"
    "v_mov_b64 v[220:221], 0\n"
    "v_mov_b64 v[222:223], 0\n"
    "v_mov_b64 v[224:225], 0\n"
    "v_mov_b64 v[226:227], 0\n"
    "v_mov_b64 v[228:229], 0\n"
    "v_mov_b64 v[230:231], 0\n"
    "v_mov_b64 v[232:233], 0\n"
    "v_mov_b64 v[234:235], 0\n"
    "v_mov_b64 v[236:237], 0\n"
    "v_mov_b64 v[238:239], 0\n"
    "v_mov_b64 v[240:241], 0\n"
    "v_mov_b64 v[242:243], 0\n"
    "v_mov_b64 v[244:245], 0\n"
    "v_mov_b64 v[246:247], 0\n"
    "v_mov_b64 v[248:249], 0\n"
    "v_mov_b64 v[250:251], 0\n"
    "v_mov_b64 v[252:253], 0\n"
    "v_mov_b64 v[254:255], 0\n"
    "s_mov_b32 %[sc], 6\n"
    "s_waitcnt lgkmcnt(0)\n"
    "s_barrier\n"
    "1:\n"
    "v_mfma_f32_16x16x32_bf16 v[192:195], v[144:147], v[128:131], v[192:195]\n"
    "ds_read_b128 v[160:163], %[vk0] offset:16640\n"
    "v_mfma_f32_16x16x32_bf16 v[208:211], v[144:147], v[132:135], v[208:211]\n"
    "ds_read_b64_tr_b16 v[176:177], %[vb0] offset:16640\n"
    "v_mfma_f32_16x16x32_bf16 v[224:227], v[144:147], v[136:139], v[224:227]\n"
    "ds_read_b128 v[164:167], %[vk0] offset:16896\n"
    "v_mfma_f32_16x16x32_bf16 v[240:243], v[144:147], v[140:143], v[240:243]\n"
    "ds_read_b64_tr_b16 v[178:179], %[vb0] offset:16896\n"
    "v_mfma_f32_16x16x32_bf16 v[196:199], v[148:151], v[128:131], v[196:199]\n"
    "ds_read_b128 v[168:171], %[vs0] offset:16640\n"
    "v_mfma_f32_16x16x32_bf16 v[212:215], v[148:151], v[132:135], v[212:215]\n"
    "ds_read_b64_tr_b16 v[180:181], %[vb0] offset:16672\n"
    "v_mfma_f32_16x16x32_bf16 v[228:231], v[148:151], v[136:139], v[228:231]\n"
    "ds_read_b128 v[172:175], %[vs0] offset:16896\n"
    "v_mfma_f32_16x16x32_bf16 v[244:247], v[148:151], v[140:143], v[244:247]\n"
    "ds_read_b64_tr_b16 v[182:183], %[vb0] offset:16928\n"
    "v_mfma_f32_16x16x32_bf16 v[200:203], v[152:155], v[128:131], v[200:203]\n"
    "ds_read_b64_tr_b16 v[184:185], %[vb0] offset:16704\n"
    "v_mfma_f32_16x16x32_bf16 v[216:219], v[152:155], v[132:135], v[216:219]\n"
    "ds_read_b64_tr_b16 v[186:187], %[vb0] offset:16960\n"
    "v_mfma_f32_16x16x32_bf16 v[232:235], v[152:155], v[136:139], v[232:235]\n"
    "ds_read_b64_tr_b16 v[188:189], %[vb0] offset:16736\n"
    "v_mfma_f32_16x16x32_bf16 v[248:251], v[152:155], v[140:143], v[248:251]\n"
    "ds_read_b64_tr_b16 v[190:191], %[vb0] offset:16992\n"
    "v_mfma_f32_16x16x32_bf16 v[204:207], v[156:159], v[128:131], v[204:207]\n"
    "v_mfma_f32_16x16x32_bf16 v[220:223], v[156:159], v[132:135], v[220:223]\n"
    "v_mfma_f32_16x16x32_bf16 v[236:239], v[156:159], v[136:139], v[236:239]\n"
    "s_waitcnt lgkmcnt(0)\n"
    "v_mfma_f32_16x16x32_bf16 v[252:255], v[156:159], v[140:143], v[252:255]\n"
    "v_mfma_f32_16x16x32_bf16 v[192:195], v[176:179], v[160:163], v[192:195]\n"
    "ds_read_b128 v[128:131], %[vk0] offset:33280\n"
    "v_mfma_f32_16x16x32_bf16 v[208:211], v[176:179], v[164:167], v[208:211]\n"
    "ds_read_b64_tr_b16 v[144:145], %[vb0] offset:33280\n"
    "v_mfma_f32_16x16x32_bf16 v[224:227], v[176:179], v[168:171], v[224:227]\n"
    "ds_read_b128 v[132:135], %[vk0] offset:33536\n"
    "v_mfma_f32_16x16x32_bf16 v[240:243], v[176:179], v[172:175], v[240:243]\n"
    "ds_read_b64_tr_b16 v[146:147], %[vb0] offset:33536\n"
    "v_mfma_f32_16x16x32_bf16 v[196:199], v[180:183], v[160:163], v[196:199]\n"
    "ds_read_b128 v[136:139], %[vs0] offset:33280\n"
    "v_mfma_f32_16x16x32_bf16 v[212:215], v[180:183], v[164:167], v[212:215]\n"
    "ds_read_b64_tr_b16 v[148:149], %[vb0] offset:33312\n"
    "v_mfma_f32_16x16x32_bf16 v[228:231], v[180:183], v[168:171], v[228:231]\n"
    "ds_read_b128 v[140:143], %[vs0] offset:33536\n"
    "v_mfma_f32_16x16x32_bf16 v[244:247], v[180:183], v[172:175], v[244:247]\n"
    "ds_read_b64_tr_b16 v[150:151], %[vb0] offset:33568\n"
    "v_mfma_f32_16x16x32_bf16 v[200:203], v[184:187], v[160:163], v[200:203]\n"
    "ds_read_b64_tr_b16 v[152:153], %[vb0] offset:33344\n"
    "v_mfma_f32_16x16x32_bf16 v[216:219], v[184:187], v[164:167], v[216:219]\n"
    "ds_read_b64_tr_b16 v[154:155], %[vb0] offset:33600\n"
    "v_mfma_f32_16x16x32_bf16 v[232:235], v[184:187], v[168:171], v[232:235]\n"
    "ds_read_b64_tr_b16 v[156:157], %[vb0] offset:33376\n"
    "v_mfma_f32_16x16x32_bf16 v[248:251], v[184:187], v[172:175], v[248:251]\n"
    "ds_read_b64_tr_b16 v[158:159], %[vb0] offset:33632\n"
    "v_mfma_f32_16x16x32_bf16 v[204:207], v[188:191], v[160:163], v[204:207]\n"
    "v_mfma_f32_16x16x32_bf16 v[220:223], v[188:191], v[164:167], v[220:223]\n"
    "v_mfma_f32_16x16x32_bf16 v[236:239], v[188:191], v[168:171], v[236:239]\n"
    "s_waitcnt lgkmcnt(0)\n"
    "v_mfma_f32_16x16x32_bf16 v[252:255], v[188:191], v[172:175], v[252:255]\n"
    "s_barrier\n"
    "v_mfma_f32_16x16x32_bf16 v[192:195], v[144:147], v[128:131], v[192:195]\n"
    "ds_read_b128 v[160:163], %[vk0] offset:49920\n"
    "v_mfma_f32_16x16x32_bf16 v[208:211], v[144:147], v[132:135], v[208:211]\n"
    "ds_read_b64_tr_b16 v[176:177], %[vb0] offset:49920\n"
    "v_mfma_f32_16x16x32_bf16 v[224:227], v[144:147], v[136:139], v[224:227]\n"
    "ds_read_b128 v[164:167], %[vk0] offset:50176\n"
    "v_mfma_f32_16x16x32_bf16 v[240:243], v[144:147], v[140:143], v[240:243]\n"
    "ds_read_b64_tr_b16 v[178:179], %[vb0] offset:50176\n"
    "v_mfma_f32_16x16x32_bf16 v[196:199], v[148:151], v[128:131], v[196:199]\n"
    "ds_read_b128 v[168:171], %[vs0] offset:49920\n"
    "v_mfma_f32_16x16x32_bf16 v[212:215], v[148:151], v[132:135], v[212:215]\n"
    "ds_read_b64_tr_b16 v[180:181], %[vb0] offset:49952\n"
    "v_mfma_f32_16x16x32_bf16 v[228:231], v[148:151], v[136:139], v[228:231]\n"
    "ds_read_b128 v[172:175], %[vs0] offset:50176\n"
    "v_mfma_f32_16x16x32_bf16 v[244:247], v[148:151], v[140:143], v[244:247]\n"
    "ds_read_b64_tr_b16 v[182:183], %[vb0] offset:50208\n"
    "v_mfma_f32_16x16x32_bf16 v[200:203], v[152:155], v[128:131], v[200:203]\n"
    "ds_read_b64_tr_b16 v[184:185], %[vb0] offset:49984\n"
    "v_mfma_f32_16x16x32_bf16 v[216:219], v[152:155], v[132:135], v[216:219]\n"
    "ds_read_b64_tr_b16 v[186:187], %[vb0] offset:50240\n"
    "v_mfma_f32_16x16x32_bf16 v[232:235], v[152:155], v[136:139], v[232:235]\n"
    "ds_read_b64_tr_b16 v[188:189], %[vb0] offset:50016\n"
    "v_mfma_f32_16x16x32_bf16 v[248:251], v[152:155], v[140:143], v[248:251]\n"
    "ds_read_b64_tr_b16 v[190:191], %[vb0] offset:50272\n"
    "v_mfma_f32_16x16x32_bf16 v[204:207], v[156:159], v[128:131], v[204:207]\n"
    "v_mfma_f32_16x16x32_bf16 v[220:223], v[156:159], v[132:135], v[220:223]\n"
    "v_mfma_f32_16x16x32_bf16 v[236:239], v[156:159], v[136:139], v[236:239]\n"
    "s_waitcnt lgkmcnt(0)\n"
    "v_mfma_f32_16x16x32_bf16 v[252:255], v[156:159], v[140:143], v[252:255]\n"
    "v_mfma_f32_16x16x32_bf16 v[192:195], v[176:179], v[160:163], v[192:195]\n"
    "ds_read_b128 v[128:131], %[vk1] offset:0\n"
    "v_mfma_f32_16x16x32_bf16 v[208:211], v[176:179], v[164:167], v[208:211]\n"
    "ds_read_b64_tr_b16 v[144:145], %[vb1] offset:0\n"
    "v_mfma_f32_16x16x32_bf16 v[224:227], v[176:179], v[168:171], v[224:227]\n"
    "ds_read_b128 v[132:135], %[vk1] offset:256\n"
    "v_mfma_f32_16x16x32_bf16 v[240:243], v[176:179], v[172:175], v[240:243]\n"
    "ds_read_b64_tr_b16 v[146:147], %[vb1] offset:256\n"
    "v_mfma_f32_16x16x32_bf16 v[196:199], v[180:183], v[160:163], v[196:199]\n"
    "ds_read_b128 v[136:139], %[vs1] offset:0\n"
    "v_mfma_f32_16x16x32_bf16 v[212:215], v[180:183], v[164:167], v[212:215]\n"
    "ds_read_b64_tr_b16 v[148:149], %[vb1] offset:32\n"
    "v_mfma_f32_16x16x32_bf16 v[228:231], v[180:183], v[168:171], v[228:231]\n"
    "ds_read_b128 v[140:143], %[vs1] offset:256\n"
    "v_mfma_f32_16x16x32_bf16 v[244:247], v[180:183], v[172:175], v[244:247]\n"
    "ds_read_b64_tr_b16 v[150:151], %[vb1] offset:288\n"
    "v_mfma_f32_16x16x32_bf16 v[200:203], v[184:187], v[160:163], v[200:203]\n"
    "ds_read_b64_tr_b16 v[152:153], %[vb1] offset:64\n"
    "v_mfma_f32_16x16x32_bf16 v[216:219], v[184:187], v[164:167], v[216:219]\n"
    "ds_read_b64_tr_b16 v[154:155], %[vb1] offset:320\n"
    "v_mfma_f32_16x16x32_bf16 v[232:235], v[184:187], v[168:171], v[232:235]\n"
    "ds_read_b64_tr_b16 v[156:157], %[vb1] offset:96\n"
    "v_mfma_f32_16x16x32_bf16 v[248:251], v[184:187], v[172:175], v[248:251]\n"
    "ds_read_b64_tr_b16 v[158:159], %[vb1] offset:352\n"
    "v_mfma_f32_16x16x32_bf16 v[204:207], v[188:191], v[160:163], v[204:207]\n"
    "v_mfma_f32_16x16x32_bf16 v[220:223], v[188:191], v[164:167], v[220:223]\n"
    "v_mfma_f32_16x16x32_bf16 v[236:239], v[188:191], v[168:171], v[236:239]\n"
    "s_waitcnt lgkmcnt(0)\n"
    "v_mfma_f32_16x16x32_bf16 v[252:255], v[188:191], v[172:175], v[252:255]\n"
    "s_barrier\n"
    "v_mfma_f32_16x16x32_bf16 v[192:195], v[144:147], v[128:131], v[192:195]\n"
    "ds_read_b128 v[160:163], %[vk1] offset:16640\n"
    "v_mfma_f32_16x16x32_bf16 v[208:211], v[144:147], v[132:135], v[208:211]\n"
    "ds_read_b64_tr_b16 v[176:177], %[vb1] offset:16640\n"
    "v_mfma_f32_16x16x32_bf16 v[224:227], v[144:147], v[136:139], v[224:227]\n"
    "ds_read_b128 v[164:167], %[vk1] offset:16896\n"
    "v_mfma_f32_16x16x32_bf16 v[240:243], v[144:147], v[140:143], v[240:243]\n"
    "ds_read_b64_tr_b16 v[178:179], %[vb1] offset:16896\n"
    "v_mfma_f32_16x16x32_bf16 v[196:199], v[148:151], v[128:131], v[196:199]\n"
    "ds_read_b128 v[168:171], %[vs1] offset:16640\n"
    "v_mfma_f32_16x16x32_bf16 v[212:215], v[148:151], v[132:135], v[212:215]\n"
    "ds_read_b64_tr_b16 v[180:181], %[vb1] offset:16672\n"
    "v_mfma_f32_16x16x32_bf16 v[228:231], v[148:151], v[136:139], v[228:231]\n"
    "ds_read_b128 v[172:175], %[vs1] offset:16896\n"
    "v_mfma_f32_16x16x32_bf16 v[244:247], v[148:151], v[140:143], v[244:247]\n"
    "ds_read_b64_tr_b16 v[182:183], %[vb1] offset:16928\n"
    "v_mfma_f32_16x16x32_bf16 v[200:203], v[152:155], v[128:131], v[200:203]\n"
    "ds_read_b64_tr_b16 v[184:185], %[vb1] offset:16704\n"
    "v_mfma_f32_16x16x32_bf16 v[216:219], v[152:155], v[132:135], v[216:219]\n"
    "ds_read_b64_tr_b16 v[186:187], %[vb1] offset:16960\n"
    "v_mfma_f32_16x16x32_bf16 v[232:235], v[152:155], v[136:139], v[232:235]\n"
    "ds_read_b64_tr_b16 v[188:189], %[vb1] offset:16736\n"
    "v_mfma_f32_16x16x32_bf16 v[248:251], v[152:155], v[140:143], v[248:251]\n"
    "ds_read_b64_tr_b16 v[190:191], %[vb1] offset:16992\n"
    "v_mfma_f32_16x16x32_bf16 v[204:207], v[156:159], v[128:131], v[204:207]\n"
    "v_mfma_f32_16x16x32_bf16 v[220:223], v[156:159], v[132:135], v[220:223]\n"
    "v_mfma_f32_16x16x32_bf16 v[236:239], v[156:159], v[136:139], v[236:239]\n"
    "s_waitcnt lgkmcnt(0)\n"
    "v_mfma_f32_16x16x32_bf16 v[252:255], v[156:159], v[140:143], v[252:255]\n"
    "v_mfma_f32_16x16x32_bf16 v[192:195], v[176:179], v[160:163], v[192:195]\n"
    "ds_read_b128 v[128:131], %[vk1] offset:33280\n"
    "v_mfma_f32_16x16x32_bf16 v[208:211], v[176:179], v[164:167], v[208:211]\n"
    "ds_read_b64_tr_b16 v[144:145], %[vb1] offset:33280\n"
    "v_mfma_f32_16x16x32_bf16 v[224:227], v[176:179], v[168:171], v[224:227]\n"
    "ds_read_b128 v[132:135], %[vk1] offset:33536\n"
    "v_mfma_f32_16x16x32_bf16 v[240:243], v[176:179], v[172:175], v[240:243]\n"
    "ds_read_b64_tr_b16 v[146:147], %[vb1] offset:33536\n"
    "v_mfma_f32_16x16x32_bf16 v[196:199], v[180:183], v[160:163], v[196:199]\n"
    "ds_read_b128 v[136:139], %[vs1] offset:33280\n"
    "v_mfma_f32_16x16x32_bf16 v[212:215], v[180:183], v[164:167], v[212:215]\n"
    "ds_read_b64_tr_b16 v[148:149], %[vb1] offset:33312\n"
    "v_mfma_f32_16x16x32_bf16 v[228:231], v[180:183], v[168:171], v[228:231]\n"
    "ds_read_b128 v[140:143], %[vs1] offset:33536\n"
    "v_mfma_f32_16x16x32_bf16 v[244:247], v[180:183], v[172:175], v[244:247]\n"
    "ds_read_b64_tr_b16 v[150:151], %[vb1] offset:33568\n"
    "v_mfma_f32_16x16x32_bf16 v[200:203], v[184:187], v[160:163], v[200:203]\n"
    "ds_read_b64_tr_b16 v[152:153], %[vb1] offset:33344\n"
    "v_mfma_f32_16x16x32_bf16 v[216:219], v[184:187], v[164:167], v[216:219]\n"
    "ds_read_b64_tr_b16 v[154:155], %[vb1] offset:33600\n"
    "v_mfma_f32_16x16x32_bf16 v[232:235], v[184:187], v[168:171], v[232:235]\n"
    "ds_read_b64_tr_b16 v[156:157], %[vb1] offset:33376\n"
    "v_mfma_f32_16x16x32_bf16 v[248:251], v[184:187], v[172:175], v[248:251]\n"
    "ds_read_b64_tr_b16 v[158:159], %[vb1] offset:33632\n"
    "v_mfma_f32_16x16x32_bf16 v[204:207], v[188:191], v[160:163], v[204:207]\n"
    "s_cmp_lg_u32 %[sc], 6\n"
    "v_mfma_f32_16x16x32_bf16 v[220:223], v[188:191], v[164:167], v[220:223]\n"
    "s_cbranch_scc1 2f\n"
    "s_waitcnt vmcnt(0)\n"
    "2:\n"
    "v_mfma_f32_16x16x32_bf16 v[236:239], v[188:191], v[168:171], v[236:239]\n"
    "s_waitcnt lgkmcnt(0)\n"
    "v_mfma_f32_16x16x32_bf16 v[252:255], v[188:191], v[172:175], v[252:255]\n"
    "s_barrier\n"
    "v_mfma_f32_16x16x32_bf16 v[192:195], v[144:147], v[128:131], v[192:195]\n"
    "ds_read_b128 v[160:163], %[vk1] offset:49920\n"
    "v_mfma_f32_16x16x32_bf16 v[208:211], v[144:147], v[132:135], v[208:211]\n"
    "ds_read_b64_tr_b16 v[176:177], %[vb1] offset:49920\n"
    "v_mfma_f32_16x16x32_bf16 v[224:227], v[144:147], v[136:139], v[224:227]\n"
    "ds_read_b128 v[164:167], %[vk1] offset:50176\n"
    "v_mfma_f32_16x16x32_bf16 v[240:243], v[144:147], v[140:143], v[240:243]\n"
    "ds_read_b64_tr_b16 v[178:179], %[vb1] offset:50176\n"
    "v_mfma_f32_16x16x32_bf16 v[196:199], v[148:151], v[128:131], v[196:199]\n"
    "ds_read_b128 v[168:171], %[vs1] offset:49920\n"
    "v_mfma_f32_16x16x32_bf16 v[212:215], v[148:151], v[132:135], v[212:215]\n"
    "ds_read_b64_tr_b16 v[180:181], %[vb1] offset:49952\n"
    "v_mfma_f32_16x16x32_bf16 v[228:231], v[148:151], v[136:139], v[228:231]\n"
    "ds_read_b128 v[172:175], %[vs1] offset:50176\n"
    "v_mfma_f32_16x16x32_bf16 v[244:247], v[148:151], v[140:143], v[244:247]\n"
    "ds_read_b64_tr_b16 v[182:183], %[vb1] offset:50208\n"
    "v_mfma_f32_16x16x32_bf16 v[200:203], v[152:155], v[128:131], v[200:203]\n"
    "ds_read_b64_tr_b16 v[184:185], %[vb1] offset:49984\n"
    "v_mfma_f32_16x16x32_bf16 v[216:219], v[152:155], v[132:135], v[216:219]\n"
    "ds_read_b64_tr_b16 v[186:187], %[vb1] offset:50240\n"
    "v_mfma_f32_16x16x32_bf16 v[232:235], v[152:155], v[136:139], v[232:235]\n"
    "ds_read_b64_tr_b16 v[188:189], %[vb1] offset:50016\n"
    "v_mfma_f32_16x16x32_bf16 v[248:251], v[152:155], v[140:143], v[248:251]\n"
    "ds_read_b64_tr_b16 v[190:191], %[vb1] offset:50272\n"
    "v_mfma_f32_16x16x32_bf16 v[204:207], v[156:159], v[128:131], v[204:207]\n"
    "s_cmp_lg_u32 %[sc], %[hk]\n"
    "v_mfma_f32_16x16x32_bf16 v[220:223], v[156:159], v[132:135], v[220:223]\n"
    "s_cbranch_scc1 3f\n"
    "s_mov_b64 exec, 1\n"
    "buffer_store_dword %[ft], off, %[ws], %[fo] sc1\n"
    "s_mov_b64 exec, -1\n"
    "3:\n"
    "v_mfma_f32_16x16x32_bf16 v[236:239], v[156:159], v[136:139], v[236:239]\n"
    "s_waitcnt lgkmcnt(0)\n"
    "v_mfma_f32_16x16x32_bf16 v[252:255], v[156:159], v[140:143], v[252:255]\n"
    "v_mfma_f32_16x16x32_bf16 v[192:195], v[176:179], v[160:163], v[192:195]\n"
    "ds_read_b128 v[128:131], %[vk0] offset:0\n"
    "v_mfma_f32_16x16x32_bf16 v[208:211], v[176:179], v[164:167], v[208:211]\n"
    "ds_read_b64_tr_b16 v[144:145], %[vb0] offset:0\n"
    "v_mfma_f32_16x16x32_bf16 v[224:227], v[176:179], v[168:171], v[224:227]\n"
    "ds_read_b128 v[132:135], %[vk0] offset:256\n"
    "v_mfma_f32_16x16x32_bf16 v[240:243], v[176:179], v[172:175], v[240:243]\n"
    "ds_read_b64_tr_b16 v[146:147], %[vb0] offset:256\n"
    "v_mfma_f32_16x16x32_bf16 v[196:199], v[180:183], v[160:163], v[196:199]\n"
    "ds_read_b128 v[136:139], %[vs0] offset:0\n"
    "v_mfma_f32_16x16x32_bf16 v[212:215], v[180:183], v[164:167], v[212:215]\n"
    "ds_read_b64_tr_b16 v[148:149], %[vb0] offset:32\n"
    "v_mfma_f32_16x16x32_bf16 v[228:231], v[180:183], v[168:171], v[228:231]\n"
    "ds_read_b128 v[140:143], %[vs0] offset:256\n"
    "v_mfma_f32_16x16x32_bf16 v[244:247], v[180:183], v[172:175], v[244:247]\n"
    "ds_read_b64_tr_b16 v[150:151], %[vb0] offset:288\n"
    "v_mfma_f32_16x16x32_bf16 v[200:203], v[184:187], v[160:163], v[200:203]\n"
    "ds_read_b64_tr_b16 v[152:153], %[vb0] offset:64\n"
    "v_mfma_f32_16x16x32_bf16 v[216:219], v[184:187], v[164:167], v[216:219]\n"
    "ds_read_b64_tr_b16 v[154:155], %[vb0] offset:320\n"
    "v_mfma_f32_16x16x32_bf16 v[232:235], v[184:187], v[168:171], v[232:235]\n"
    "ds_read_b64_tr_b16 v[156:157], %[vb0] offset:96\n"
    "v_mfma_f32_16x16x32_bf16 v[248:251], v[184:187], v[172:175], v[248:251]\n"
    "ds_read_b64_tr_b16 v[158:159], %[vb0] offset:352\n"
    "v_mfma_f32_16x16x32_bf16 v[204:207], v[188:191], v[160:163], v[204:207]\n"
    "v_mfma_f32_16x16x32_bf16 v[220:223], v[188:191], v[164:167], v[220:223]\n"
    "v_mfma_f32_16x16x32_bf16 v[236:239], v[188:191], v[168:171], v[236:239]\n"
    "s_waitcnt lgkmcnt(0)\n"
    "v_mfma_f32_16x16x32_bf16 v[252:255], v[188:191], v[172:175], v[252:255]\n"
    "s_barrier\n"
    "s_sub_u32 %[sc], %[sc], 1\n"
    "s_cbranch_scc0 1b\n"
    "s_nop 7\n"
        : "=&{v[192:223]}"(keep), "=&{v[224:255]}"(send), [sc] "=&s"(sc)
        : [vk0] "v"(vk), [vk1] "v"(vk + 4 * SL4), [vs0] "v"(vs), [vs1] "v"(vs + 4 * SL4), [vb0] "v"(vb), [vb1] "v"(vb + 4 * SL4), [ws] "s"(ws),
          [fo] "s"(fo + LV_L2 * 32768), [ft] "v"(tagv + LV_L2), [fp] "s"(fo + LV_L1 * 32768), [f1] "v"(tagv + LV_L1), [hk] "s"(wave ? -1 : 6)
        : "memory", "scc", "v128", "v129", "v130", "v131", "v132", "v133", "v134", "v135", "v136", "v137", "v138", "v139", "v140", "v141", "v142", "v143", "v144", "v145", "v146", "v147", "v148", "v149", "v150", "v151", "v152", "v153", "v154", "v155", "v156", "v157", "v158", "v159", "v160", "v161", "v162", "v163", "v164", "v165", "v166", "v167", "v168", "v169", "v170", "v171", "v172", "v173", "v174", "v175", "v176", "v177", "v178", "v179", "v180", "v181", "v182", "v183", "v184", "v185", "v186", "v187", "v188", "v189", "v190", "v191");
    STAMP(13);
#pragma unroll
    for (int i = 0; i < 4; ++i)
      st16<AUX_SC1>(rw, lane * 16 + i * 1024, WS_XCH + ((blockIdx.x ^ 1) * 4 + wave) * 4096,
                    (u32x4){min(pack2(pair_at(send, 8 * i)), XCH_EMPTY - 1), min(pack2(pair_at(send, 8 * i + 2)), XCH_EMPTY - 1),
                            min(pack2(pair_at(send, 8 * i + 4)), XCH_EMPTY - 1), min(pack2(pair_at(send, 8 * i + 6)), XCH_EMPTY - 1)});
    FENCE();
    f32x2 xv[2][4][2], cv[2][4][2], zv[2][4][2];
#pragma unroll
    for (int f = 0; f < 4; ++f) {
      const u32x4 yw = *L<u32x4>(L_Y + (wc * 16 + f * 4 + g) * 512 + wr * 256 + mm * 16);
#pragma unroll
      for (int m = 0; m < 2; ++m) {
        const int rl = wr * 32 + m * 16 + mm;
        const f32x2 st = *L<f32x2>(L_ST1 + (kh * 64 + rl) * 8);
        const u32x2 xw = *L<u32x2>(rl * 256 + (((wc * 8 + f * 2 + (g >> 1)) ^ mm) << 4) + (g & 1) * 8);
        const u32x2 x1w = *L<u32x2>(SL4 + (wc * 8 + f * 2 + (g >> 1)) * 1024 + rl * 16 + (g & 1) * 8);
#pragma unroll
        for (int h = 0; h < 2; ++h) {
          xv[m][f][h] = bf_pair(xw[h]);
          cv[m][f][h] = fma2(xv[m][f][h], splat(st.y), splat(st.x)) * bf_pair(yw[2 * m + h]);
          zv[m][f][h] = pair_at(keep, 16 * m + 4 * f + 2 * h) + bf_pair(x1w[h]);
        }
      }
    }
    FENCE();
    u32x4 pw[4];
    for (int spin = 0; spin < SPIN; ++spin) {
      unsigned mx = 0;
      asm volatile("" ::: "memory");
#pragma unroll
      for (int i = 0; i < 4; ++i) {
        pw[i] = ld16<AUX_SC1>(rw, lane * 16 + i * 1024, WS_XCH + (blockIdx.x * 4 + wave) * 4096);
        mx = max(max(mx, max(pw[i][0], pw[i][1])), max(pw[i][2], pw[i][3]));
      }
      if (__builtin_amdgcn_ballot_w64(mx == XCH_EMPTY) == 0) break;
    }
    STAMP(14);
    const unsigned ot = 3 * SL4 + wave * 4096;
    const rsrc_t ro = mk_rsrc(out, S * D * 2);
#pragma unroll
    for (int m = 0; m < 2; ++m) {
#pragma unroll
      for (int f = 0; f < 4; ++f) {
        u32x2 o;
#pragma unroll
        for (int h = 0; h < 2; ++h) {
          const f32x2 z = zv[m][f][h] + bf_pair(pw[m * 2 + (f >> 1)][(f & 1) * 2 + h]);
          const f32x2 zl = z * splat(-1.4426950408889634f);
          const f32x2 e = (f32x2){__builtin_amdgcn_exp2f(zl.x), __builtin_amdgcn_exp2f(zl.y)} + splat(1.0f);
          o[h] = pack2(fma2(cv[m][f][h] * z, (f32x2){__builtin_amdgcn_rcpf(e.x), __builtin_amdgcn_rcpf(e.y)}, xv[m][f][h]));
        }
        *L<u32x2>(ot + (m * 16 + mm) * 128 + (((f * 2 + (g >> 1)) ^ (mm & 7)) << 4) + (g & 1) * 8) = o;
      }
#pragma unroll
      for (int i = 0; i < 2; ++i) {
        const int rl = m * 16 + i * 8 + (lane >> 3);
        st16<16>(ro, rl * 2048 + (lane & 7) * 16, (pm * 128 + kh * 64 + wr * 32) * 2048 + j8 * 256 + wc * 128,
                *L<u32x4>(ot + rl * 128 + (((lane & 7) ^ (rl & 7)) << 4)));
      }
    }
  }
  PROF_END();
}

extern "C" void kernel_launch(void* const* d_in, const int* in_sizes, int n_in, void* d_out, int out_size, void* d_ws, size_t ws_size,
                              hipStream_t stream) {
  (void)hipFuncSetAttribute((const void*)k_all, hipFuncAttributeMaxDynamicSharedMemorySize, LDS_BYTES);
  k_all<<<256, 512, LDS_BYTES, stream>>>((const bf16_t*)d_in[0], (const bf16_t*)d_in[1], (const bf16_t*)d_in[2], (const bf16_t*)d_in[3],
                                         (const bf16_t*)d_in[4], (const bf16_t*)d_in[5], (const bf16_t*)d_in[7], (const bf16_t*)d_in[8],
                                         (const bf16_t*)d_in[9], (const bf16_t*)d_in[10], (char*)d_ws, (bf16_t*)d_out);
}
```
